```python
import torch

def _softmax(x, axis=-1):
    return torch.softmax(x, dim=axis)
def _gelu(x):
    return torch.nn.functional.gelu(x, approximate="tanh")
def _silu(x):
    return torch.nn.functional.silu(x)
def _sigmoid(x):
    return torch.sigmoid(x)
def _layernorm(x, axis=-1, eps=1e-5):
    m = x.mean(dim=axis, keepdim=True)
    v = x.var(dim=axis, keepdim=True, unbiased=False)
    return (x - m) / torch.sqrt(v + eps)
def _scatter_add(tgt, idx, src, axis=0):
    return tgt.clone().index_add_(axis, idx, src)
def _segment_sum(src, seg, n):
    out = torch.zeros((n,) + src.shape[1:], dtype=src.dtype)
    return out.index_add_(0, seg, src)
def _pad(x, pw):
    flat = tuple(v for pair in reversed(pw) for v in pair)
    return torch.nn.functional.pad(x, flat)

kernel_name = 'standalone__kernel_systems_refs_featgraph__graph'

def setup_inputs():

    _w = (lambda a: (lambda t: t.to(torch.bfloat16) if t.is_floating_point() else t)(torch.as_tensor(a)))
    N, deg, D = 50000, 16, 64
    E = N * deg
    scale = D ** -0.5
    src = (lambda lo, hi, *s: torch.randint(lo, hi, s))(0, N, E).to(torch.int64)
    dst = (lambda lo, hi, *s: torch.randint(lo, hi, s))(0, N, E).to(torch.int64)
    return {
        "x": _w((lambda *s: torch.randn(*s, dtype=torch.bfloat16) * 0.8)(N, D)),
        "src": _w(src),
        "dst": _w(dst),
        "w_q": _w((lambda *s: torch.randn(*s, dtype=torch.bfloat16) * 0.8)(D, D) * scale),
        "w_v": _w((lambda *s: torch.randn(*s, dtype=torch.bfloat16) * 0.05)(D, D) * scale),
    }

def reference(x, src, dst, w_q, w_v):
    N, D = x.shape
    xs = x[src]
    xd = x[dst]
    q = xd @ w_q
    logits = torch.einsum('ed,ed->e', xs, q) * (float(D) ** -0.5)
    alpha = _sigmoid(logits)
    msg = alpha[:, None] * (xs @ w_v)
    return _segment_sum(msg, dst, N)
```

```cpp
#include <hip/hip_runtime.h>
#include <hip/hip_bf16.h>
#include <stdint.h>

typedef __attribute__((ext_vector_type(2))) __bf16 bf16x2_t;
typedef __attribute__((ext_vector_type(8))) short  bf16x8_t;
typedef __attribute__((ext_vector_type(4))) unsigned u32x4;
typedef __attribute__((ext_vector_type(2))) unsigned u32x2;
typedef __attribute__((ext_vector_type(4))) int      i32x4;
typedef __attribute__((ext_vector_type(4))) float    f32x4;
typedef __attribute__((ext_vector_type(2))) float    f32x2;

static constexpr int kD        = 64;
static constexpr int kRows     = 208;
static constexpr int kMaxBN    = 207;
static constexpr int kTrash    = 207;
static constexpr int kAccStr   = 68;
static constexpr int kCap      = 8192;
static constexpr int kMaxNB    = 1024;
static constexpr int kMaxEC    = 4096;
static constexpr int kMaxNC    = 256;
static constexpr int kGroups   = 128;

__device__ __forceinline__ float dot2(unsigned a, unsigned b, float acc) {
  return __builtin_amdgcn_fdot2_f32_bf16(__builtin_bit_cast(bf16x2_t, a), __builtin_bit_cast(bf16x2_t, b), acc, false);
}
__device__ __forceinline__ unsigned cvtpk(float lo, float hi) {
  const f32x2 v = {lo, hi};
  return __builtin_bit_cast(unsigned, __builtin_convertvector(v, bf16x2_t));
}
template <int CTRL> __device__ __forceinline__ float dpp(float x) {
  return __builtin_bit_cast(float, __builtin_amdgcn_mov_dpp(__builtin_bit_cast(int, x), CTRL, 0xf, 0xf, true));
}
__device__ __forceinline__ float group8_sum(float d) {
  d += dpp<0xB1>(d);
  d += dpp<0x4E>(d);
  d += dpp<0x141>(d);
  return d;
}
__device__ __forceinline__ unsigned alpha_pk(float d) {
  const float lg = __builtin_bit_cast(float, cvtpk(d, d) << 16);
  const float z  = lg * (-0.125f * 1.4426950408889634f);
  const float ex = __builtin_amdgcn_exp2f(z);
  const float a  = __builtin_amdgcn_rcpf(1.0f + ex);
  return cvtpk(a, 0.0f);
}

__global__ __launch_bounds__(1024) void k_bucket(const int* __restrict__ src, const int* __restrict__ dst,
                                                 int E, int EC, int BN, unsigned magic, int NB,
                                                 unsigned* __restrict__ elist, unsigned* __restrict__ table) {
  __shared__ __attribute__((aligned(16))) unsigned hist[kMaxNB];
  __shared__ __attribute__((aligned(16))) unsigned sorted[kMaxEC];
  const int c = blockIdx.x, t = threadIdx.x;
  const int base = c * EC;
  int n = E - base; n = n < EC ? n : EC; n = n < 0 ? 0 : n;
  if (t < NB) hist[t] = 0u;
  const int i0 = t * 4;
  int dv[4], sv[4];
  if (i0 + 3 < n) {
    const i32x4 d4 = *reinterpret_cast<const i32x4*>(dst + base + i0);
    const i32x4 s4 = *reinterpret_cast<const i32x4*>(src + base + i0);
    dv[0] = d4.x; dv[1] = d4.y; dv[2] = d4.z; dv[3] = d4.w;
    sv[0] = s4.x; sv[1] = s4.y; sv[2] = s4.z; sv[3] = s4.w;
  } else {
#pragma unroll
    for (int k = 0; k < 4; ++k) {
      const bool ok = (i0 + k) < n;
      dv[k] = ok ? dst[base + i0 + k] : -1;
      sv[k] = ok ? src[base + i0 + k] : 0;
    }
  }
  __syncthreads();
  unsigned bk[4], rk[4], ent[4];
#pragma unroll
  for (int k = 0; k < 4; ++k) {
    if (dv[k] >= 0) {
      bk[k] = __umulhi((unsigned)dv[k], magic);
      ent[k] = ((unsigned)sv[k] << 8) | ((unsigned)dv[k] - bk[k] * (unsigned)BN);
      rk[k] = atomicAdd(&hist[bk[k]], 1u);
    }
  }
  __syncthreads();
  if (t < 64) {
    const int per = (NB + 63) >> 6;
    const int b0 = t * per;
    unsigned s = 0;
    for (int k = 0; k < per; ++k) { const int b = b0 + k; s += (b < NB) ? hist[b] : 0u; }
    unsigned inc = s;
#pragma unroll
    for (int o = 1; o < 64; o <<= 1) { const unsigned v = __shfl_up(inc, o, 64); if (t >= o) inc += v; }
    unsigned run = inc - s;
    for (int k = 0; k < per; ++k) {
      const int b = b0 + k;
      if (b < NB) { const unsigned cnt = hist[b]; hist[b] = run; table[(size_t)c * NB + b] = run | (cnt << 16); run += cnt; }
    }
  }
  __syncthreads();
#pragma unroll
  for (int k = 0; k < 4; ++k)
    if (dv[k] >= 0) sorted[hist[bk[k]] + rk[k]] = ent[k];
  __syncthreads();
  if (i0 + 3 < n) {
    *reinterpret_cast<u32x4*>(elist + base + i0) = *reinterpret_cast<const u32x4*>(sorted + i0);
  } else {
#pragma unroll
    for (int k = 0; k < 4; ++k) if (i0 + k < n) elist[base + i0 + k] = sorted[i0 + k];
  }
}

struct __attribute__((aligned(16))) MainLds {
  float    acc[kRows * kAccStr];
  unsigned q[kRows * 32];
  unsigned short wqT[64 * 64];
  unsigned short wvT[64 * 64];
  unsigned list[kCap + kGroups + 16];
  unsigned cntA[kRows + 48];
  unsigned cntB[kRows + 48];
  unsigned total;
};

__device__ __forceinline__ void flush_acc(float* accrow_p, float (&acc)[8]) {
#pragma unroll
  for (int j = 0; j < 8; ++j) { atomicAdd(accrow_p + 8 * j, acc[j]); acc[j] = 0.0f; }
}

__global__ __launch_bounds__(1024) void k_main(const unsigned short* __restrict__ x,
                                               const unsigned short* __restrict__ wq,
                                               const unsigned short* __restrict__ wv,
                                               const unsigned* __restrict__ elist,
                                               const unsigned* __restrict__ table,
                                               int N, int BN, int NB, int NC, int EC,
                                               unsigned short* __restrict__ out) {
  __shared__ MainLds L;
  const int t = threadIdx.x, lane = t & 63;
  const int wave = __builtin_amdgcn_readfirstlane(t >> 6);
  int b;
  {
    const int qn = NB >> 3, rn = NB & 7, xc = blockIdx.x & 7, ix = blockIdx.x >> 3;
    b = (xc < rn ? xc * (qn + 1) : rn * (qn + 1) + (xc - rn) * qn) + ix;
  }
  const int node0 = b * BN;
  int nn = N - node0; nn = nn < BN ? nn : BN;

  const int c = t >> 2, qd = t & 3;
  const unsigned tv = (c < NC) ? table[(size_t)c * NB + b] : 0u;
  u32x4 xa0, xa1;
  const int mn = lane & 15, kq = lane >> 4;
  if (wave < 13) {
    int row = node0 + wave * 16 + mn; row = row < N ? row : N - 1;
    const unsigned short* xp = x + (size_t)row * kD + kq * 8;
    xa0 = *reinterpret_cast<const u32x4*>(xp);
    xa1 = *reinterpret_cast<const u32x4*>(xp + 32);
  }
  const u32x2 wq4 = *reinterpret_cast<const u32x2*>(wq + t * 4);
  const u32x2 wv4 = *reinterpret_cast<const u32x2*>(wv + t * 4);
  {
    f32x4 z = {0.f, 0.f, 0.f, 0.f};
    f32x4* a4 = reinterpret_cast<f32x4*>(L.acc);
    for (int i = t; i < kRows * kAccStr / 4; i += 1024) a4[i] = z;
    if (t < 256) { L.cntA[t] = 0u; L.cntB[t] = 0u; }
  }
  {
    const int k = t >> 4, f0 = (t & 15) * 4;
    const int kp = 8 * (k & 7) + (k >> 3);
    L.wqT[(f0 + 0) * 64 + k] = (unsigned short)(wq4.x & 0xffffu);
    L.wqT[(f0 + 1) * 64 + k] = (unsigned short)(wq4.x >> 16);
    L.wqT[(f0 + 2) * 64 + k] = (unsigned short)(wq4.y & 0xffffu);
    L.wqT[(f0 + 3) * 64 + k] = (unsigned short)(wq4.y >> 16);
    L.wvT[(f0 + 0) * 64 + kp] = (unsigned short)(wv4.x & 0xffffu);
    L.wvT[(f0 + 1) * 64 + kp] = (unsigned short)(wv4.x >> 16);
    L.wvT[(f0 + 2) * 64 + kp] = (unsigned short)(wv4.y & 0xffffu);
    L.wvT[(f0 + 3) * 64 + kp] = (unsigned short)(wv4.y >> 16);
  }
  const int slen = (int)(tv >> 16);
  const unsigned* seg = elist + (size_t)c * EC + (tv & 0xffffu);
  unsigned ev[8], rk[8];
#pragma unroll
  for (int i = 0; i < 8; ++i) ev[i] = (qd + 4 * i < slen) ? seg[qd + 4 * i] : 0xffffffffu;
  __syncthreads();

  if (wave < 13) {
    f32x4 cq[4];
#pragma unroll
    for (int tt = 0; tt < 4; ++tt) {
      const int f = 16 * (mn >> 2) + 4 * tt + (mn & 3);
      const bf16x8_t a0 = *reinterpret_cast<const bf16x8_t*>(&L.wqT[f * 64 + kq * 8]);
      const bf16x8_t a1 = *reinterpret_cast<const bf16x8_t*>(&L.wqT[f * 64 + 32 + kq * 8]);
      f32x4 z = {0.f, 0.f, 0.f, 0.f};
      z = __builtin_amdgcn_mfma_f32_16x16x32_bf16(a0, __builtin_bit_cast(bf16x8_t, xa0), z, 0, 0, 0);
      z = __builtin_amdgcn_mfma_f32_16x16x32_bf16(a1, __builtin_bit_cast(bf16x8_t, xa1), z, 0, 0, 0);
      cq[tt] = z;
    }
    u32x4 p0, p1;
    p0.x = cvtpk(cq[0][0], cq[0][1]); p0.y = cvtpk(cq[0][2], cq[0][3]);
    p0.z = cvtpk(cq[1][0], cq[1][1]); p0.w = cvtpk(cq[1][2], cq[1][3]);
    p1.x = cvtpk(cq[2][0], cq[2][1]); p1.y = cvtpk(cq[2][2], cq[2][3]);
    p1.z = cvtpk(cq[3][0], cq[3][1]); p1.w = cvtpk(cq[3][2], cq[3][3]);
    u32x4* qp = reinterpret_cast<u32x4*>(&L.q[(wave * 16 + mn) * 32 + kq * 8]);
    qp[0] = p0; qp[1] = p1;
  }
#pragma unroll
  for (int i = 0; i < 8; ++i)
    if (ev[i] != 0xffffffffu) rk[i] = atomicAdd(&L.cntA[ev[i] & 255u], 1u);
  for (int j = 32 + qd; j < slen; j += 4) atomicAdd(&L.cntB[seg[j] & 255u], 1u);
  __syncthreads();
  if (t < 64) {
    unsigned ca[4], cb[4], s = 0;
#pragma unroll
    for (int k = 0; k < 4; ++k) { ca[k] = L.cntA[t * 4 + k]; cb[k] = L.cntB[t * 4 + k]; s += ca[k] + cb[k]; }
    unsigned inc = s;
#pragma unroll
    for (int o = 1; o < 64; o <<= 1) { const unsigned v = __shfl_up(inc, o, 64); if (t >= o) inc += v; }
    unsigned run = inc - s;
#pragma unroll
    for (int k = 0; k < 4; ++k) { L.cntA[t * 4 + k] = run; L.cntB[t * 4 + k] = run + ca[k]; run += ca[k] + cb[k]; }
    if (t == 63) L.total = inc;
  }
  __syncthreads();
  const int total = (int)L.total;
  const int p = lane & 7;
  const int g = t >> 3;
  const unsigned p16 = (unsigned)p * 16u;
  const unsigned char* xb8 = reinterpret_cast<const unsigned char*>(x);
  const unsigned char* q8 = reinterpret_cast<const unsigned char*>(L.q);

  if (total <= kCap) {
#pragma unroll
    for (int i = 0; i < 8; ++i)
      if (ev[i] != 0xffffffffu) L.list[L.cntA[ev[i] & 255u] + rk[i]] = ev[i];
    for (int j = 32 + qd; j < slen; j += 4) {
      const unsigned e = seg[j];
      L.list[atomicAdd(&L.cntB[e & 255u], 1u)] = e;
    }
    const int Lg = (total + kGroups - 1) / kGroups;
    if (total + t < Lg * kGroups + 16) L.list[total + t] = (unsigned)kTrash;
    __syncthreads();

    const unsigned* lp = L.list + g * Lg;
    float acc[8];
#pragma unroll
    for (int j = 0; j < 8; ++j) acc[j] = 0.0f;
    unsigned ent[8]; u32x4 xv[8];
#pragma unroll
    for (int i = 0; i < 8; ++i) {
      ent[i] = (i < Lg) ? lp[i] : (unsigned)kTrash;
      xv[i] = *reinterpret_cast<const u32x4*>(xb8 + (((ent[i] >> 1) & 0x7fffff80u) | p16));
    }
    unsigned cur = ent[0] & 255u;
    for (int t0 = 0; t0 < Lg; t0 += 8) {
      unsigned nx[8];
#pragma unroll
      for (int i = 0; i < 8; ++i) nx[i] = (t0 + 8 + i < Lg) ? lp[t0 + 8 + i] : (unsigned)kTrash;
#pragma unroll
      for (int i = 0; i < 8; ++i) {
        const unsigned e = ent[i];
        const u32x4 xr = xv[i];
        ent[i] = nx[i];
        xv[i] = *reinterpret_cast<const u32x4*>(xb8 + (((nx[i] >> 1) & 0x7fffff80u) | p16));
        if (t0 + i < Lg) {
          const unsigned dl = e & 255u;
          const u32x4 qv = *reinterpret_cast<const u32x4*>(q8 + (dl * 128u + p16));
          if (dl != cur) { flush_acc(&L.acc[cur * kAccStr + p], acc); cur = dl; }
          float d = dot2(xr.x, qv.x, 0.0f);
          d = dot2(xr.y, qv.y, d); d = dot2(xr.z, qv.z, d); d = dot2(xr.w, qv.w, d);
          d = group8_sum(d);
          const unsigned al = alpha_pk(d), ah = al << 16;
          acc[0] = dot2(xr.x, al, acc[0]); acc[1] = dot2(xr.x, ah, acc[1]);
          acc[2] = dot2(xr.y, al, acc[2]); acc[3] = dot2(xr.y, ah, acc[3]);
          acc[4] = dot2(xr.z, al, acc[4]); acc[5] = dot2(xr.z, ah, acc[5]);
          acc[6] = dot2(xr.w, al, acc[6]); acc[7] = dot2(xr.w, ah, acc[7]);
        }
      }
    }
    flush_acc(&L.acc[cur * kAccStr + p], acc);
  } else {
    for (int cc = 0; cc < NC; ++cc) {
      const unsigned tw = table[(size_t)cc * NB + b];
      const int len = (int)(tw >> 16);
      const unsigned* sg = elist + (size_t)cc * EC + (tw & 0xffffu);
      for (int j = g; j < len; j += kGroups) {
        const unsigned e = sg[j];
        const unsigned dl = e & 255u;
        const u32x4 xr = *reinterpret_cast<const u32x4*>(xb8 + (((e >> 1) & 0x7fffff80u) | p16));
        const u32x4 qv = *reinterpret_cast<const u32x4*>(q8 + (dl * 128u + p16));
        float d = dot2(xr.x, qv.x, 0.0f);
        d = dot2(xr.y, qv.y, d); d = dot2(xr.z, qv.z, d); d = dot2(xr.w, qv.w, d);
        d = group8_sum(d);
        const unsigned al = alpha_pk(d), ah = al << 16;
        float acc[8];
        acc[0] = dot2(xr.x, al, 0.f); acc[1] = dot2(xr.x, ah, 0.f);
        acc[2] = dot2(xr.y, al, 0.f); acc[3] = dot2(xr.y, ah, 0.f);
        acc[4] = dot2(xr.z, al, 0.f); acc[5] = dot2(xr.z, ah, 0.f);
        acc[6] = dot2(xr.w, al, 0.f); acc[7] = dot2(xr.w, ah, 0.f);
        flush_acc(&L.acc[dl * kAccStr + p], acc);
      }
    }
  }
  __syncthreads();

  if (wave * 16 < nn) {
    const int row = wave * 16 + mn;
    const float* sp = &L.acc[row * kAccStr + kq * 8];
    bf16x8_t bh[2], bl[2];
#pragma unroll
    for (int s = 0; s < 2; ++s) {
      const f32x4 s0 = *reinterpret_cast<const f32x4*>(sp + 32 * s);
      const f32x4 s1 = *reinterpret_cast<const f32x4*>(sp + 32 * s + 4);
      const float sv[8] = {s0[0], s0[1], s0[2], s0[3], s1[0], s1[1], s1[2], s1[3]};
      u32x4 h, l;
#pragma unroll
      for (int k = 0; k < 4; ++k) {
        const unsigned hp = cvtpk(sv[2 * k], sv[2 * k + 1]);
        const float f0 = __builtin_bit_cast(float, hp << 16), f1 = __builtin_bit_cast(float, hp & 0xffff0000u);
        h[k] = hp; l[k] = cvtpk(sv[2 * k] - f0, sv[2 * k + 1] - f1);
      }
      bh[s] = __builtin_bit_cast(bf16x8_t, h); bl[s] = __builtin_bit_cast(bf16x8_t, l);
    }
    f32x4 co[4];
#pragma unroll
    for (int tt = 0; tt < 4; ++tt) {
      const int f = 16 * (mn >> 2) + 4 * tt + (mn & 3);
      const bf16x8_t a0 = *reinterpret_cast<const bf16x8_t*>(&L.wvT[f * 64 + kq * 8]);
      const bf16x8_t a1 = *reinterpret_cast<const bf16x8_t*>(&L.wvT[f * 64 + 32 + kq * 8]);
      f32x4 z = {0.f, 0.f, 0.f, 0.f};
      z = __builtin_amdgcn_mfma_f32_16x16x32_bf16(a0, bl[0], z, 0, 0, 0);
      z = __builtin_amdgcn_mfma_f32_16x16x32_bf16(a1, bl[1], z, 0, 0, 0);
      z = __builtin_amdgcn_mfma_f32_16x16x32_bf16(a0, bh[0], z, 0, 0, 0);
      z = __builtin_amdgcn_mfma_f32_16x16x32_bf16(a1, bh[1], z, 0, 0, 0);
      co[tt] = z;
    }
    if (row < nn) {
      u32x4 p0, p1;
      p0.x = cvtpk(co[0][0], co[0][1]); p0.y = cvtpk(co[0][2], co[0][3]);
      p0.z = cvtpk(co[1][0], co[1][1]); p0.w = cvtpk(co[1][2], co[1][3]);
      p1.x = cvtpk(co[2][0], co[2][1]); p1.y = cvtpk(co[2][2], co[2][3]);
      p1.z = cvtpk(co[3][0], co[3][1]); p1.w = cvtpk(co[3][2], co[3][3]);
      u32x4* op = reinterpret_cast<u32x4*>(out + (size_t)(node0 + row) * kD + kq * 16);
      op[0] = p0; op[1] = p1;
    }
  }
}

__device__ __forceinline__ float bf2f(unsigned short v) { return __builtin_bit_cast(float, (unsigned)v << 16); }

__global__ __launch_bounds__(256) void f_prep(const unsigned short* __restrict__ x, const unsigned short* __restrict__ wq,
                                              int N, unsigned short* __restrict__ Q, float* __restrict__ S) {
  const size_t i = (size_t)blockIdx.x * 256 + threadIdx.x;
  if (i >= (size_t)N * kD) return;
  const size_t n = i >> 6; const int f = (int)(i & 63);
  float a = 0.f;
  for (int k = 0; k < kD; ++k) a = fmaf(bf2f(x[n * kD + k]), bf2f(wq[k * kD + f]), a);
  Q[i] = (unsigned short)(cvtpk(a, a) & 0xffffu);
  S[i] = 0.f;
}
__global__ __launch_bounds__(256) void f_edge(const unsigned short* __restrict__ x, const unsigned short* __restrict__ Q,
                                              const int* __restrict__ src, const int* __restrict__ dst, int E,
                                              float* __restrict__ S) {
  const size_t gid = ((size_t)blockIdx.x * 256 + threadIdx.x) >> 3;
  const int p = threadIdx.x & 7;
  const bool ok = gid < (size_t)E;
  const size_t e = ok ? gid : 0;
  const int s = src[e], d = dst[e];
  const u32x4 xr = *reinterpret_cast<const u32x4*>(x + (size_t)s * kD + p * 8);
  const u32x4 qv = *reinterpret_cast<const u32x4*>(Q + (size_t)d * kD + p * 8);
  float dd = dot2(xr.x, qv.x, 0.0f);
  dd = dot2(xr.y, qv.y, dd); dd = dot2(xr.z, qv.z, dd); dd = dot2(xr.w, qv.w, dd);
  dd = group8_sum(dd);
  const unsigned al = alpha_pk(dd), ah = al << 16;
  if (ok) {
    float* o = S + (size_t)d * kD + p * 8;
    atomicAdd(o + 0, dot2(xr.x, al, 0.f)); atomicAdd(o + 1, dot2(xr.x, ah, 0.f));
    atomicAdd(o + 2, dot2(xr.y, al, 0.f)); atomicAdd(o + 3, dot2(xr.y, ah, 0.f));
    atomicAdd(o + 4, dot2(xr.z, al, 0.f)); atomicAdd(o + 5, dot2(xr.z, ah, 0.f));
    atomicAdd(o + 6, dot2(xr.w, al, 0.f)); atomicAdd(o + 7, dot2(xr.w, ah, 0.f));
  }
}
__global__ __launch_bounds__(256) void f_out(const float* __restrict__ S, const unsigned short* __restrict__ wv,
                                             int N, unsigned short* __restrict__ out) {
  const size_t i = (size_t)blockIdx.x * 256 + threadIdx.x;
  if (i >= (size_t)N * kD) return;
  const size_t n = i >> 6; const int f = (int)(i & 63);
  float a = 0.f;
  for (int k = 0; k < kD; ++k) a = fmaf(S[n * kD + k], bf2f(wv[k * kD + f]), a);
  out[i] = (unsigned short)(cvtpk(a, a) & 0xffffu);
}

extern "C" void kernel_launch(void* const* d_in, const int* in_sizes, int n_in,
                              void* d_out, int out_size, void* d_ws, size_t ws_size,
                              hipStream_t stream) {
  const unsigned short* x  = (const unsigned short*)d_in[0];
  const int* src = (const int*)d_in[1];
  const int* dst = (const int*)d_in[2];
  const unsigned short* wq = (const unsigned short*)d_in[3];
  const unsigned short* wv = (const unsigned short*)d_in[4];
  unsigned short* out = (unsigned short*)d_out;
  const int N = in_sizes[0] / kD;
  const int E = in_sizes[1];
  if (N <= 0) return;

  int BN = (N + 255) / 256; if (BN > kMaxBN) BN = kMaxBN; if (BN < 2) BN = 2;
  const int NB = (N + BN - 1) / BN;
  int NC = kMaxNC;
  int EC = (E + NC - 1) / NC; EC = (EC + 3) & ~3; if (EC < 4) EC = 4;
  const bool fast = (NB <= kMaxNB) && (EC <= kMaxEC) && (N <= (1 << 24)) && (E > 0);
  if (fast) {
    NC = (E + EC - 1) / EC;
    unsigned* elist = (unsigned*)d_ws;
    unsigned* table = elist + (((size_t)NC * EC + 64 + 63) & ~(size_t)63);
    const unsigned magic = (unsigned)((0x100000000ull / (unsigned)BN) + 1ull);
    k_bucket<<<NC, 1024, 0, stream>>>(src, dst, E, EC, BN, magic, NB, elist, table);
    k_main<<<NB, 1024, 0, stream>>>(x, wq, wv, elist, table, N, BN, NB, NC, EC, out);
  } else {
    float* S = (float*)d_ws;
    unsigned short* Q = (unsigned short*)(S + (size_t)N * kD);
    const unsigned gn = (unsigned)(((size_t)N * kD + 255) / 256);
    f_prep<<<gn, 256, 0, stream>>>(x, wq, N, Q, S);
    if (E > 0) f_edge<<<(unsigned)(((size_t)E * 8 + 255) / 256), 256, 0, stream>>>(x, Q, src, dst, E, S);
    f_out<<<gn, 256, 0, stream>>>(S, wv, N, out);
  }
}
```

```cpp
#include <hip/hip_runtime.h>
#include <hip/hip_bf16.h>
#include <stdint.h>

typedef __attribute__((ext_vector_type(2))) __bf16 bf16x2_t;
typedef __attribute__((ext_vector_type(8))) short  bf16x8_t;
typedef __attribute__((ext_vector_type(4))) unsigned u32x4;
typedef __attribute__((ext_vector_type(2))) unsigned u32x2;
typedef __attribute__((ext_vector_type(4))) int      i32x4;
typedef __attribute__((ext_vector_type(4))) float    f32x4;
typedef __attribute__((ext_vector_type(2))) float    f32x2;

static constexpr int kD        = 64;
static constexpr int kRows     = 208;
static constexpr int kMaxBN    = 207;
static constexpr int kTrash    = 207;
static constexpr int kAccStr   = 68;
static constexpr int kCap      = 8192;
static constexpr int kMaxNB    = 1024;
static constexpr int kMaxEC    = 4096;
static constexpr int kMaxNC    = 256;
static constexpr int kGroups   = 128;

__device__ __forceinline__ float dot2(unsigned a, unsigned b, float acc) {
  return __builtin_amdgcn_fdot2_f32_bf16(__builtin_bit_cast(bf16x2_t, a), __builtin_bit_cast(bf16x2_t, b), acc, false);
}
__device__ __forceinline__ unsigned cvtpk(float lo, float hi) {
  const f32x2 v = {lo, hi};
  return __builtin_bit_cast(unsigned, __builtin_convertvector(v, bf16x2_t));
}
template <int CTRL> __device__ __forceinline__ float dpp(float x) {
  return __builtin_bit_cast(float, __builtin_amdgcn_mov_dpp(__builtin_bit_cast(int, x), CTRL, 0xf, 0xf, true));
}
__device__ __forceinline__ float group8_sum(float d) {
  d += dpp<0xB1>(d);
  d += dpp<0x4E>(d);
  d += dpp<0x141>(d);
  return d;
}
__device__ __forceinline__ unsigned alpha_pk(float d) {
  const float lg = __builtin_bit_cast(float, cvtpk(d, d) << 16);
  const float z  = lg * (-0.125f * 1.4426950408889634f);
  const float ex = __builtin_amdgcn_exp2f(z);
  const float a  = __builtin_amdgcn_rcpf(1.0f + ex);
  return cvtpk(a, 0.0f);
}

__global__ __launch_bounds__(1024) void k_bucket(const int* __restrict__ src, const int* __restrict__ dst,
                                                 int E, int EC, int BN, unsigned magic, int NB,
                                                 unsigned* __restrict__ elist, unsigned* __restrict__ table) {
  __shared__ __attribute__((aligned(16))) unsigned hist[kMaxNB];
  __shared__ __attribute__((aligned(16))) unsigned sorted[kMaxEC];
  const int c = blockIdx.x, t = threadIdx.x;
  const int base = c * EC;
  int n = E - base; n = n < EC ? n : EC; n = n < 0 ? 0 : n;
  if (t < NB) hist[t] = 0u;
  const int i0 = t * 4;
  int dv[4], sv[4];
  if (i0 + 3 < n) {
    const i32x4 d4 = *reinterpret_cast<const i32x4*>(dst + base + i0);
    const i32x4 s4 = *reinterpret_cast<const i32x4*>(src + base + i0);
    dv[0] = d4.x; dv[1] = d4.y; dv[2] = d4.z; dv[3] = d4.w;
    sv[0] = s4.x; sv[1] = s4.y; sv[2] = s4.z; sv[3] = s4.w;
  } else {
#pragma unroll
    for (int k = 0; k < 4; ++k) {
      const bool ok = (i0 + k) < n;
      dv[k] = ok ? dst[base + i0 + k] : -1;
      sv[k] = ok ? src[base + i0 + k] : 0;
    }
  }
  __syncthreads();
  unsigned bk[4], rk[4], ent[4];
#pragma unroll
  for (int k = 0; k < 4; ++k) {
    if (dv[k] >= 0) {
      bk[k] = __umulhi((unsigned)dv[k], magic);
      ent[k] = ((unsigned)sv[k] << 8) | ((unsigned)dv[k] - bk[k] * (unsigned)BN);
      rk[k] = atomicAdd(&hist[bk[k]], 1u);
    }
  }
  __syncthreads();
  if (t < 64) {
    const int per = (NB + 63) >> 6;
    const int b0 = t * per;
    unsigned s = 0;
    for (int k = 0; k < per; ++k) { const int b = b0 + k; s += (b < NB) ? hist[b] : 0u; }
    unsigned inc = s;
#pragma unroll
    for (int o = 1; o < 64; o <<= 1) { const unsigned v = __shfl_up(inc, o, 64); if (t >= o) inc += v; }
    unsigned run = inc - s;
    for (int k = 0; k < per; ++k) {
      const int b = b0 + k;
      if (b < NB) { const unsigned cnt = hist[b]; hist[b] = run; table[(size_t)c * NB + b] = run | (cnt << 16); run += cnt; }
    }
  }
  __syncthreads();
#pragma unroll
  for (int k = 0; k < 4; ++k)
    if (dv[k] >= 0) sorted[hist[bk[k]] + rk[k]] = ent[k];
  __syncthreads();
  if (i0 + 3 < n) {
    *reinterpret_cast<u32x4*>(elist + base + i0) = *reinterpret_cast<const u32x4*>(sorted + i0);
  } else {
#pragma unroll
    for (int k = 0; k < 4; ++k) if (i0 + k < n) elist[base + i0 + k] = sorted[i0 + k];
  }
}

static constexpr int kSlots = kRows + kGroups;
struct __attribute__((aligned(16))) MainLds {
  float    acc[kSlots * kAccStr];
  unsigned q[kRows * 32];
  unsigned short wvT[64 * 64];
  union {
    unsigned short wqT[64 * 64];
    unsigned list[kCap + kGroups + 16];
  };
  unsigned cntA[kRows + 48];
  unsigned cntB[kRows + 48];
  unsigned total;
};

__device__ __forceinline__ void flush_acc(float* slot_p, float (&acc)[8]) {
  f32x4 a = {acc[0], acc[1], acc[2], acc[3]}, b = {acc[4], acc[5], acc[6], acc[7]};
  reinterpret_cast<f32x4*>(slot_p)[0] = a; reinterpret_cast<f32x4*>(slot_p)[1] = b;
#pragma unroll
  for (int j = 0; j < 8; ++j) acc[j] = 0.0f;
}

__global__ __launch_bounds__(1024) void k_main(const unsigned short* __restrict__ x,
                                               const unsigned short* __restrict__ wq,
                                               const unsigned short* __restrict__ wv,
                                               const unsigned* __restrict__ elist,
                                               const unsigned* __restrict__ table,
                                               int N, int BN, int NB, int NC, int EC,
                                               unsigned short* __restrict__ out) {
  __shared__ MainLds L;
#define STAMP(i)
  STAMP(0);
  const int t = threadIdx.x, lane = t & 63;
  const int wave = __builtin_amdgcn_readfirstlane(t >> 6);
  int b;
  {
    const int qn = NB >> 3, rn = NB & 7, xc = blockIdx.x & 7, ix = blockIdx.x >> 3;
    b = (xc < rn ? xc * (qn + 1) : rn * (qn + 1) + (xc - rn) * qn) + ix;
  }
  const int node0 = b * BN;
  int nn = N - node0; nn = nn < BN ? nn : BN;

  const int c = t >> 2, qd = t & 3;
  const unsigned tv = (c < NC) ? table[(size_t)c * NB + b] : 0u;
  u32x4 xa0, xa1;
  const int mn = lane & 15, kq = lane >> 4;
  if (wave < 13) {
    int row = node0 + wave * 16 + mn; row = row < N ? row : N - 1;
    const unsigned short* xp = x + (size_t)row * kD + kq * 8;
    xa0 = *reinterpret_cast<const u32x4*>(xp);
    xa1 = *reinterpret_cast<const u32x4*>(xp + 32);
  }
  const u32x2 wq4 = *reinterpret_cast<const u32x2*>(wq + t * 4);
  const u32x2 wv4 = *reinterpret_cast<const u32x2*>(wv + t * 4);
  {
    if (t < 256) { L.cntA[t] = 0u; L.cntB[t] = 0u; }
  }
  {
    const int k = t >> 4, f0 = (t & 15) * 4;
    const int kp = k;
    L.wqT[(f0 + 0) * 64 + k] = (unsigned short)(wq4.x & 0xffffu);
    L.wqT[(f0 + 1) * 64 + k] = (unsigned short)(wq4.x >> 16);
    L.wqT[(f0 + 2) * 64 + k] = (unsigned short)(wq4.y & 0xffffu);
    L.wqT[(f0 + 3) * 64 + k] = (unsigned short)(wq4.y >> 16);
    L.wvT[(f0 + 0) * 64 + kp] = (unsigned short)(wv4.x & 0xffffu);
    L.wvT[(f0 + 1) * 64 + kp] = (unsigned short)(wv4.x >> 16);
    L.wvT[(f0 + 2) * 64 + kp] = (unsigned short)(wv4.y & 0xffffu);
    L.wvT[(f0 + 3) * 64 + kp] = (unsigned short)(wv4.y >> 16);
  }
  const int slen = (int)(tv >> 16);
  const unsigned* seg = elist + (size_t)c * EC + (tv & 0xffffu);
  unsigned ev[8], rk[8];
#pragma unroll
  for (int i = 0; i < 8; ++i) ev[i] = (qd + 4 * i < slen) ? seg[qd + 4 * i] : 0xffffffffu;
  __syncthreads();
  STAMP(1);

  if (wave < 13) {
    f32x4 cq[4];
#pragma unroll
    for (int tt = 0; tt < 4; ++tt) {
      const int f = 16 * (mn >> 2) + 4 * tt + (mn & 3);
      const bf16x8_t a0 = *reinterpret_cast<const bf16x8_t*>(&L.wqT[f * 64 + kq * 8]);
      const bf16x8_t a1 = *reinterpret_cast<const bf16x8_t*>(&L.wqT[f * 64 + 32 + kq * 8]);
      f32x4 z = {0.f, 0.f, 0.f, 0.f};
      z = __builtin_amdgcn_mfma_f32_16x16x32_bf16(a0, __builtin_bit_cast(bf16x8_t, xa0), z, 0, 0, 0);
      z = __builtin_amdgcn_mfma_f32_16x16x32_bf16(a1, __builtin_bit_cast(bf16x8_t, xa1), z, 0, 0, 0);
      cq[tt] = z;
    }
    u32x4 p0, p1;
    p0.x = cvtpk(cq[0][0], cq[0][1]); p0.y = cvtpk(cq[0][2], cq[0][3]);
    p0.z = cvtpk(cq[1][0], cq[1][1]); p0.w = cvtpk(cq[1][2], cq[1][3]);
    p1.x = cvtpk(cq[2][0], cq[2][1]); p1.y = cvtpk(cq[2][2], cq[2][3]);
    p1.z = cvtpk(cq[3][0], cq[3][1]); p1.w = cvtpk(cq[3][2], cq[3][3]);
    u32x4* qp = reinterpret_cast<u32x4*>(&L.q[(wave * 16 + mn) * 32 + kq * 8]);
    qp[0] = p0; qp[1] = p1;
  }
#pragma unroll
  for (int i = 0; i < 8; ++i)
    if (ev[i] != 0xffffffffu) rk[i] = atomicAdd(&L.cntA[ev[i] & 255u], 1u);
  for (int j = 32 + qd; j < slen; j += 4) atomicAdd(&L.cntB[seg[j] & 255u], 1u);
  __syncthreads();
  STAMP(2);
  if (t < 64) {
    unsigned ca[4], cb[4], s = 0;
#pragma unroll
    for (int k = 0; k < 4; ++k) { ca[k] = L.cntA[t * 4 + k]; cb[k] = L.cntB[t * 4 + k]; s += ca[k] + cb[k]; }
    unsigned inc = s;
#pragma unroll
    for (int o = 1; o < 64; o <<= 1) { const unsigned v = __shfl_up(inc, o, 64); if (t >= o) inc += v; }
    unsigned run = inc - s;
#pragma unroll
    for (int k = 0; k < 4; ++k) { L.cntA[t * 4 + k] = run; L.cntB[t * 4 + k] = run + ca[k]; run += ca[k] + cb[k]; }
    if (t == 63) L.total = inc;
  }
  __syncthreads();
  STAMP(3);
  const int total = (int)L.total;
  const int p = lane & 7;
  const int g = t >> 3;
  const unsigned p16 = (unsigned)p * 16u;
  const unsigned char* xb8 = reinterpret_cast<const unsigned char*>(x);
  const unsigned char* q8 = reinterpret_cast<const unsigned char*>(L.q);

  int Lge = 0;
  if (total <= kCap) {
#pragma unroll
    for (int i = 0; i < 8; ++i)
      if (ev[i] != 0xffffffffu) L.list[L.cntA[ev[i] & 255u] + rk[i]] = ev[i];
    for (int j = 32 + qd; j < slen; j += 4) {
      const unsigned e = seg[j];
      L.list[atomicAdd(&L.cntB[e & 255u], 1u)] = e;
    }
    const int Lg = total > 0 ? (total + kGroups - 1) / kGroups : 1;
    if (total + t < Lg * kGroups + 16) L.list[total + t] = (unsigned)kTrash;
    __syncthreads();
    STAMP(4);

    const unsigned* lp = L.list + g * Lg;
    float acc[8];
#pragma unroll
    for (int j = 0; j < 8; ++j) acc[j] = 0.0f;
    unsigned ent[8]; u32x4 xv[8];
#pragma unroll
    for (int i = 0; i < 8; ++i) {
      ent[i] = (i < Lg) ? lp[i] : (unsigned)kTrash;
      xv[i] = *reinterpret_cast<const u32x4*>(xb8 + (((ent[i] >> 1) & 0x7fffff80u) | p16));
    }
    unsigned cur = ent[0] & 255u;
    for (int t0 = 0; t0 < Lg; t0 += 8) {
      unsigned nx[8];
#pragma unroll
      for (int i = 0; i < 8; ++i) nx[i] = (t0 + 8 + i < Lg) ? lp[t0 + 8 + i] : (unsigned)kTrash;
#pragma unroll
      for (int i = 0; i < 8; ++i) {
        const unsigned e = ent[i];
        const u32x4 xr = xv[i];
        ent[i] = nx[i];
        xv[i] = *reinterpret_cast<const u32x4*>(xb8 + (((nx[i] >> 1) & 0x7fffff80u) | p16));
        if (t0 + i < Lg) {
          const unsigned dl = e & 255u;
          const u32x4 qv = *reinterpret_cast<const u32x4*>(q8 + (dl * 128u + p16));
          if (dl != cur) { flush_acc(&L.acc[(cur + g) * kAccStr + p * 8], acc); cur = dl; }
          float d = dot2(xr.x, qv.x, 0.0f);
          d = dot2(xr.y, qv.y, d); d = dot2(xr.z, qv.z, d); d = dot2(xr.w, qv.w, d);
          d = group8_sum(d);
          const unsigned al = alpha_pk(d), ah = al << 16;
          acc[0] = dot2(xr.x, al, acc[0]); acc[1] = dot2(xr.x, ah, acc[1]);
          acc[2] = dot2(xr.y, al, acc[2]); acc[3] = dot2(xr.y, ah, acc[3]);
          acc[4] = dot2(xr.z, al, acc[4]); acc[5] = dot2(xr.z, ah, acc[5]);
          acc[6] = dot2(xr.w, al, acc[6]); acc[7] = dot2(xr.w, ah, acc[7]);
        }
      }
    }
    flush_acc(&L.acc[(cur + g) * kAccStr + p * 8], acc);
    Lge = Lg;
    STAMP(5);
  } else {
    for (int i = t; i < kRows * kAccStr; i += 1024) L.acc[i] = 0.0f;
    __syncthreads();
    for (int cc = 0; cc < NC; ++cc) {
      const unsigned tw = table[(size_t)cc * NB + b];
      const int len = (int)(tw >> 16);
      const unsigned* sg = elist + (size_t)cc * EC + (tw & 0xffffu);
      for (int j = g; j < len; j += kGroups) {
        const unsigned e = sg[j];
        const unsigned dl = e & 255u;
        const u32x4 xr = *reinterpret_cast<const u32x4*>(xb8 + (((e >> 1) & 0x7fffff80u) | p16));
        const u32x4 qv = *reinterpret_cast<const u32x4*>(q8 + (dl * 128u + p16));
        float d = dot2(xr.x, qv.x, 0.0f);
        d = dot2(xr.y, qv.y, d); d = dot2(xr.z, qv.z, d); d = dot2(xr.w, qv.w, d);
        d = group8_sum(d);
        const unsigned al = alpha_pk(d), ah = al << 16;
        float acc[8];
        acc[0] = dot2(xr.x, al, 0.f); acc[1] = dot2(xr.x, ah, 0.f);
        acc[2] = dot2(xr.y, al, 0.f); acc[3] = dot2(xr.y, ah, 0.f);
        acc[4] = dot2(xr.z, al, 0.f); acc[5] = dot2(xr.z, ah, 0.f);
        acc[6] = dot2(xr.w, al, 0.f); acc[7] = dot2(xr.w, ah, 0.f);
#pragma unroll
        for (int j2 = 0; j2 < 8; ++j2) atomicAdd(&L.acc[dl * kAccStr + p * 8 + j2], acc[j2]);
      }
    }
  }
  __syncthreads();
  STAMP(6);

  if (wave * 16 < nn) {
    const int row = wave * 16 + mn;
    int g0 = 0, g1 = 0;
    if (Lge > 0) {
      const int s0i = (int)L.cntA[row], s1i = (int)L.cntA[row + 1];
      g0 = s0i / Lge; g1 = (s1i > s0i) ? (s1i - 1) / Lge : g0 - 1;
    }
    float sa[16];
#pragma unroll
    for (int k = 0; k < 16; ++k) sa[k] = 0.0f;
    for (int gg = g0; gg <= g1; ++gg) {
      const float* sp = &L.acc[(row + gg) * kAccStr + kq * 8];
      const f32x4 v0 = *reinterpret_cast<const f32x4*>(sp), v1 = *reinterpret_cast<const f32x4*>(sp + 4);
      const f32x4 v2 = *reinterpret_cast<const f32x4*>(sp + 32), v3 = *reinterpret_cast<const f32x4*>(sp + 36);
#pragma unroll
      for (int k = 0; k < 4; ++k) { sa[k] += v0[k]; sa[4 + k] += v1[k]; sa[8 + k] += v2[k]; sa[12 + k] += v3[k]; }
    }
    bf16x8_t bh[2], bl[2];
#pragma unroll
    for (int s = 0; s < 2; ++s) {
      const float* sv = sa + 8 * s;
      u32x4 h, l;
#pragma unroll
      for (int k = 0; k < 4; ++k) {
        const unsigned hp = cvtpk(sv[2 * k], sv[2 * k + 1]);
        const float f0 = __builtin_bit_cast(float, hp << 16), f1 = __builtin_bit_cast(float, hp & 0xffff0000u);
        h[k] = hp; l[k] = cvtpk(sv[2 * k] - f0, sv[2 * k + 1] - f1);
      }
      bh[s] = __builtin_bit_cast(bf16x8_t, h); bl[s] = __builtin_bit_cast(bf16x8_t, l);
    }
    f32x4 co[4];
#pragma unroll
    for (int tt = 0; tt < 4; ++tt) {
      const int f = 16 * (mn >> 2) + 4 * tt + (mn & 3);
      const bf16x8_t a0 = *reinterpret_cast<const bf16x8_t*>(&L.wvT[f * 64 + kq * 8]);
      const bf16x8_t a1 = *reinterpret_cast<const bf16x8_t*>(&L.wvT[f * 64 + 32 + kq * 8]);
      f32x4 z = {0.f, 0.f, 0.f, 0.f};
      z = __builtin_amdgcn_mfma_f32_16x16x32_bf16(a0, bl[0], z, 0, 0, 0);
      z = __builtin_amdgcn_mfma_f32_16x16x32_bf16(a1, bl[1], z, 0, 0, 0);
      z = __builtin_amdgcn_mfma_f32_16x16x32_bf16(a0, bh[0], z, 0, 0, 0);
      z = __builtin_amdgcn_mfma_f32_16x16x32_bf16(a1, bh[1], z, 0, 0, 0);
      co[tt] = z;
    }
    if (row < nn) {
      u32x4 p0, p1;
      p0.x = cvtpk(co[0][0], co[0][1]); p0.y = cvtpk(co[0][2], co[0][3]);
      p0.z = cvtpk(co[1][0], co[1][1]); p0.w = cvtpk(co[1][2], co[1][3]);
      p1.x = cvtpk(co[2][0], co[2][1]); p1.y = cvtpk(co[2][2], co[2][3]);
      p1.z = cvtpk(co[3][0], co[3][1]); p1.w = cvtpk(co[3][2], co[3][3]);
      u32x4* op = reinterpret_cast<u32x4*>(out + (size_t)(node0 + row) * kD + kq * 16);
      op[0] = p0; op[1] = p1;
    }
  }
  STAMP(7);
}

__device__ __forceinline__ float bf2f(unsigned short v) { return __builtin_bit_cast(float, (unsigned)v << 16); }

__global__ __launch_bounds__(256) void f_prep(const unsigned short* __restrict__ x, const unsigned short* __restrict__ wq,
                                              int N, unsigned short* __restrict__ Q, float* __restrict__ S) {
  const size_t i = (size_t)blockIdx.x * 256 + threadIdx.x;
  if (i >= (size_t)N * kD) return;
  const size_t n = i >> 6; const int f = (int)(i & 63);
  float a = 0.f;
  for (int k = 0; k < kD; ++k) a = fmaf(bf2f(x[n * kD + k]), bf2f(wq[k * kD + f]), a);
  Q[i] = (unsigned short)(cvtpk(a, a) & 0xffffu);
  S[i] = 0.f;
}
__global__ __launch_bounds__(256) void f_edge(const unsigned short* __restrict__ x, const unsigned short* __restrict__ Q,
                                              const int* __restrict__ src, const int* __restrict__ dst, int E,
                                              float* __restrict__ S) {
  const size_t gid = ((size_t)blockIdx.x * 256 + threadIdx.x) >> 3;
  const int p = threadIdx.x & 7;
  const bool ok = gid < (size_t)E;
  const size_t e = ok ? gid : 0;
  const int s = src[e], d = dst[e];
  const u32x4 xr = *reinterpret_cast<const u32x4*>(x + (size_t)s * kD + p * 8);
  const u32x4 qv = *reinterpret_cast<const u32x4*>(Q + (size_t)d * kD + p * 8);
  float dd = dot2(xr.x, qv.x, 0.0f);
  dd = dot2(xr.y, qv.y, dd); dd = dot2(xr.z, qv.z, dd); dd = dot2(xr.w, qv.w, dd);
  dd = group8_sum(dd);
  const unsigned al = alpha_pk(dd), ah = al << 16;
  if (ok) {
    float* o = S + (size_t)d * kD + p * 8;
    atomicAdd(o + 0, dot2(xr.x, al, 0.f)); atomicAdd(o + 1, dot2(xr.x, ah, 0.f));
    atomicAdd(o + 2, dot2(xr.y, al, 0.f)); atomicAdd(o + 3, dot2(xr.y, ah, 0.f));
    atomicAdd(o + 4, dot2(xr.z, al, 0.f)); atomicAdd(o + 5, dot2(xr.z, ah, 0.f));
    atomicAdd(o + 6, dot2(xr.w, al, 0.f)); atomicAdd(o + 7, dot2(xr.w, ah, 0.f));
  }
}
__global__ __launch_bounds__(256) void f_out(const float* __restrict__ S, const unsigned short* __restrict__ wv,
                                             int N, unsigned short* __restrict__ out) {
  const size_t i = (size_t)blockIdx.x * 256 + threadIdx.x;
  if (i >= (size_t)N * kD) return;
  const size_t n = i >> 6; const int f = (int)(i & 63);
  float a = 0.f;
  for (int k = 0; k < kD; ++k) a = fmaf(S[n * kD + k], bf2f(wv[k * kD + f]), a);
  out[i] = (unsigned short)(cvtpk(a, a) & 0xffffu);
}

extern "C" void kernel_launch(void* const* d_in, const int* in_sizes, int n_in,
                              void* d_out, int out_size, void* d_ws, size_t ws_size,
                              hipStream_t stream) {
  const unsigned short* x  = (const unsigned short*)d_in[0];
  const int* src = (const int*)d_in[1];
  const int* dst = (const int*)d_in[2];
  const unsigned short* wq = (const unsigned short*)d_in[3];
  const unsigned short* wv = (const unsigned short*)d_in[4];
  unsigned short* out = (unsigned short*)d_out;
  const int N = in_sizes[0] / kD;
  const int E = in_sizes[1];
  if (N <= 0) return;

  int BN = (N + 255) / 256; if (BN > kMaxBN) BN = kMaxBN; if (BN < 2) BN = 2;
  const int NB = (N + BN - 1) / BN;
  int NC = kMaxNC;
  int EC = (E + NC - 1) / NC; EC = (EC + 3) & ~3; if (EC < 4) EC = 4;
  const bool fast = (NB <= kMaxNB) && (EC <= kMaxEC) && (N <= (1 << 24)) && (E > 0);
  if (fast) {
    NC = (E + EC - 1) / EC;
    unsigned* elist = (unsigned*)d_ws;
    unsigned* table = elist + (((size_t)NC * EC + 64 + 63) & ~(size_t)63);
    const unsigned magic = (unsigned)((0x100000000ull / (unsigned)BN) + 1ull);
    k_bucket<<<NC, 1024, 0, stream>>>(src, dst, E, EC, BN, magic, NB, elist, table);
    k_main<<<NB, 1024, 0, stream>>>(x, wq, wv, elist, table, N, BN, NB, NC, EC, out);
  } else {
    float* S = (float*)d_ws;
    unsigned short* Q = (unsigned short*)(S + (size_t)N * kD);
    const unsigned gn = (unsigned)(((size_t)N * kD + 255) / 256);
    f_prep<<<gn, 256, 0, stream>>>(x, wq, N, Q, S);
    if (E > 0) f_edge<<<(unsigned)(((size_t)E * 8 + 255) / 256), 256, 0, stream>>>(x, Q, src, dst, E, S);
    f_out<<<gn, 256, 0, stream>>>(S, wv, N, out);
  }
}
```

```cpp
#define SELFB 1
#define SELFM 1
#define CPS 64u
#include <hip/hip_runtime.h>
#include <hip/hip_bf16.h>
#include <stdint.h>

typedef __attribute__((ext_vector_type(2))) __bf16 bf16x2_t;
typedef __attribute__((ext_vector_type(8))) short  bf16x8_t;
typedef __attribute__((ext_vector_type(4))) short  bf16x4_t;
typedef __attribute__((ext_vector_type(4))) unsigned u32x4;
typedef __attribute__((ext_vector_type(2))) unsigned u32x2;
typedef __attribute__((ext_vector_type(4))) int      i32x4;
typedef __attribute__((ext_vector_type(4))) float    f32x4;
typedef __attribute__((ext_vector_type(2))) float    f32x2;

#define FEAT(h, kq, j) (32 * (h) + 8 * (kq) + (j))
#define DEPTH 3
static constexpr int kD        = 64;
static constexpr int kMaxBN    = 256;
static constexpr int kMaxNB    = 256;
static constexpr int kMaxEC    = 4096;
static constexpr int kMaxNC    = 256;
static constexpr unsigned kDummy = 0xff000000u;

__device__ __forceinline__ float dot2(unsigned a, unsigned b, float acc) {
  return __builtin_amdgcn_fdot2_f32_bf16(__builtin_bit_cast(bf16x2_t, a), __builtin_bit_cast(bf16x2_t, b), acc, false);
}
__device__ __forceinline__ unsigned cvtpk(float lo, float hi) {
  const f32x2 v = {lo, hi};
  return __builtin_bit_cast(unsigned, __builtin_convertvector(v, bf16x2_t));
}
template <int CTRL> __device__ __forceinline__ float dpp(float x) {
  return __builtin_bit_cast(float, __builtin_amdgcn_mov_dpp(__builtin_bit_cast(int, x), CTRL, 0xf, 0xf, true));
}
__device__ __forceinline__ float group8_sum(float d) {
  d += dpp<0xB1>(d);
  d += dpp<0x4E>(d);
  d += dpp<0x141>(d);
  return d;
}
__device__ __forceinline__ unsigned wave_incl_scan(unsigned v) {
  v += (unsigned)__builtin_amdgcn_update_dpp(0, (int)v, 0x111, 0xf, 0xf, true);
  v += (unsigned)__builtin_amdgcn_update_dpp(0, (int)v, 0x112, 0xf, 0xf, true);
  v += (unsigned)__builtin_amdgcn_update_dpp(0, (int)v, 0x114, 0xf, 0xf, true);
  v += (unsigned)__builtin_amdgcn_update_dpp(0, (int)v, 0x118, 0xf, 0xf, true);
  v += (unsigned)__builtin_amdgcn_update_dpp(0, (int)v, 0x142, 0xa, 0xf, false);
  v += (unsigned)__builtin_amdgcn_update_dpp(0, (int)v, 0x143, 0xc, 0xf, false);
  return v;
}
__device__ __forceinline__ unsigned alpha_pk(float d) {
  const float lg = __builtin_bit_cast(float, cvtpk(d, d) << 16);
  const float z  = lg * (-0.125f * 1.4426950408889634f);
  const float ex = __builtin_amdgcn_exp2f(z);
  const float a  = __builtin_amdgcn_rcpf(1.0f + ex);
  return cvtpk(a, 0.0f);
}
__device__ __forceinline__ unsigned alpha2(float d0, float d1) {
  const unsigned pk = cvtpk(d0, d1);
  f32x2 lg = {__builtin_bit_cast(float, pk << 16), __builtin_bit_cast(float, pk & 0xffff0000u)};
  const f32x2 cz = {-0.125f * 1.4426950408889634f, -0.125f * 1.4426950408889634f};
  lg = lg * cz;
  f32x2 ex = {__builtin_amdgcn_exp2f(lg.x), __builtin_amdgcn_exp2f(lg.y)};
  const f32x2 one = {1.0f, 1.0f};
  ex = ex + one;
  return cvtpk(__builtin_amdgcn_rcpf(ex.x), __builtin_amdgcn_rcpf(ex.y));
}
__device__ __forceinline__ f32x4 mfma32(const u32x4& a, const u32x4& b, const f32x4& c) {
  return __builtin_amdgcn_mfma_f32_16x16x32_bf16(__builtin_bit_cast(bf16x8_t, a), __builtin_bit_cast(bf16x8_t, b), c, 0, 0, 0);
}
__device__ __forceinline__ f32x4 mfma16(const u32x2& a, const u32x2& b, const f32x4& c) {
  return __builtin_amdgcn_mfma_f32_16x16x16bf16_1k(__builtin_bit_cast(bf16x4_t, a), __builtin_bit_cast(bf16x4_t, b), c, 0, 0, 0);
}

__global__ __launch_bounds__(1024) void k_bucket(const int* __restrict__ src, const int* __restrict__ dst,
                                                 const unsigned short* __restrict__ wq, const unsigned short* __restrict__ wv,
                                                 int E, int EC, int BN, unsigned magic, int NB, int NC,
                                                 u32x4* __restrict__ cells, unsigned* __restrict__ covf,
                                                 u32x4* __restrict__ wfrag, const unsigned short* __restrict__ x, int N) {
  __shared__ __attribute__((aligned(16))) unsigned img[kMaxNB * 32 + 4];
  __shared__ __attribute__((aligned(16))) unsigned hist[kMaxNB];
  __shared__ __attribute__((aligned(16))) unsigned ostart[kMaxNB];
  __shared__ unsigned flag;
#define STAMPB(i)
  STAMPB(0);
  const int c = blockIdx.x, t = threadIdx.x, lane = t & 63;
  unsigned spf = 0u;
  if (t < 64) {
    unsigned long long pc;
    asm volatile("s_getpc_b64 %0" : "=s"(pc));
    const unsigned char* cpp = reinterpret_cast<const unsigned char*>(pc) + (unsigned)(lane < 32 ? lane : 31) * 64u;
    asm volatile("global_load_dword %0, %1, off" : "=v"(spf) : "v"(cpp) : "memory");
  }
  const int base = c * EC;
  int n = E - base; n = n < EC ? n : EC; n = n < 0 ? 0 : n;
  const int i0 = t * 4;
  const bool full = i0 + 3 < n;
  i32x4 d4, s4;
  {
    const int* dp = dst + (full ? base + i0 : 0);
    const int* sp = src + (full ? base + i0 : 0);
    asm volatile("global_load_dwordx4 %0, %1, off" : "=v"(d4) : "v"(dp) : "memory");
    asm volatile("global_load_dwordx4 %0, %1, off" : "=v"(s4) : "v"(sp) : "memory");
  }
#define PFX 0
#define PFS 64
  unsigned cpf = 0u;
  if (t >= 896 && t < 960) {
    unsigned long long pc; int rel;
    asm volatile("s_getpc_b64 %0\n\ts_mov_b32 %1, k_main@rel32@lo+4" : "=&s"(pc), "=&s"(rel));
    const unsigned char* cpp = reinterpret_cast<const unsigned char*>(pc) + rel + (unsigned)lane * CPS;
    asm volatile("global_load_dword %0, %1, off" : "=v"(cpf) : "v"(cpp) : "memory");
  }
  if (t < kMaxNB) { hist[t] = 1u; ostart[t] = 0u; }
  if (t == kMaxNB) flag = 0u;
  {
    const u32x4 dd = {kDummy, kDummy, kDummy, kDummy};
    reinterpret_cast<u32x4*>(img)[t] = dd; reinterpret_cast<u32x4*>(img)[1024 + t] = dd;
  }
  asm volatile("s_waitcnt lgkmcnt(0)\n\ts_barrier" ::: "memory");
  STAMPB(1);
  if (__builtin_expect(t >= 960 && c < 16, 0)) {
    for (int fb = c; fb < 16; fb += NC) {
      const bool isq = fb < 8;
      const unsigned short* w = isq ? wq : wv;
      const int tt = (fb >> 1) & 3, sx = fb & 1, m = lane & 15, kg = lane >> 4;
      const int f = isq ? FEAT(tt >> 1, m >> 2, 4 * (tt & 1) + (m & 3)) : 16 * (m >> 2) + 4 * tt + (m & 3);
      unsigned h[8];
#pragma unroll
      for (int j = 0; j < 8; ++j) {
        const int k = isq ? FEAT(sx, kg, j) : 16 * (2 * sx + (j >> 2)) + 4 * kg + (j & 3);
        h[j] = w[k * kD + f];
      }
      u32x4 o; o.x = h[0] | (h[1] << 16); o.y = h[2] | (h[3] << 16); o.z = h[4] | (h[5] << 16); o.w = h[6] | (h[7] << 16);
      wfrag[fb * 64 + lane] = o;
    }
  }
  asm volatile("s_waitcnt vmcnt(0)" : "+v"(d4), "+v"(s4), "+v"(cpf), "+v"(spf) :: "memory");
  STAMPB(4);
  unsigned bk[4] = {0u, 0u, 0u, 0u}, rk[4] = {0u, 0u, 0u, 0u}, ent[4] = {0u, 0u, 0u, 0u};
  auto mkent = [&](unsigned d, unsigned sidx, unsigned bkt) -> unsigned {
    const unsigned dl = d - bkt * (unsigned)BN;
    const unsigned win = __umulhi(dl << 4, magic);
    const unsigned pos = dl - ((win * (unsigned)BN + 15u) >> 4);
    return (((win << 2) | (pos >> 2)) << 24) | (sidx << 7) | ((pos & 3u) << 4);
  };
  auto slot = [&](unsigned bkt, unsigned r) -> unsigned {
    return r < 32u ? bkt * 32u + ((((r >> 4) ^ bkt) & 1u) * 16u + (r & 3u) * 4u + ((r >> 2) & 3u)) : (unsigned)(kMaxNB * 32);
  };
  if (__builtin_expect(full, 1)) {
#pragma unroll
    for (int k = 0; k < 4; ++k) bk[k] = __umulhi((unsigned)d4[k], magic);
#pragma unroll
    for (int k = 0; k < 4; ++k) rk[k] = atomicAdd(&hist[bk[k]], 1u);
#pragma unroll
    for (int k = 0; k < 4; ++k) ent[k] = mkent((unsigned)d4[k], (unsigned)s4[k], bk[k]);
    STAMPB(8);
#pragma unroll
    for (int k = 0; k < 4; ++k) img[slot(bk[k], rk[k])] = ent[k];
  } else if (__builtin_expect(i0 < n, 0)) {
#pragma unroll
    for (int k = 0; k < 3; ++k) {
      if (i0 + k < n) {
        const unsigned d = (unsigned)dst[base + i0 + k];
        bk[k] = __umulhi(d, magic);
        rk[k] = atomicAdd(&hist[bk[k]], 1u);
        ent[k] = mkent(d, (unsigned)src[base + i0 + k], bk[k]);
        img[slot(bk[k], rk[k])] = ent[k];
      }
    }
  }
  {
    const unsigned m01 = rk[0] > rk[1] ? rk[0] : rk[1], m23 = rk[2] > rk[3] ? rk[2] : rk[3];
    if ((m01 > m23 ? m01 : m23) >= 32u) flag = 1u;
  }
  STAMPB(9);
  __syncthreads();
  STAMPB(2);
  if (__builtin_expect(flag != 0u, 0)) {
    if (t < 64) {
      const u32x4 h4 = *reinterpret_cast<const u32x4*>(&hist[lane * 4]);
      const unsigned o0 = h4.x > 32u ? h4.x - 32u : 0u, o1 = h4.y > 32u ? h4.y - 32u : 0u;
      const unsigned o2 = h4.z > 32u ? h4.z - 32u : 0u, o3 = h4.w > 32u ? h4.w - 32u : 0u;
      const unsigned sm = o0 + o1 + o2 + o3;
      const unsigned ex = wave_incl_scan(sm) - sm;
      u32x4 st; st.x = ex; st.y = ex + o0; st.z = ex + o0 + o1; st.w = ex + o0 + o1 + o2;
      *reinterpret_cast<u32x4*>(&ostart[lane * 4]) = st;
    }
    __syncthreads();
#pragma unroll
    for (int k = 0; k < 4; ++k)
      if (rk[k] >= 32u) covf[(size_t)base + ostart[bk[k]] + rk[k] - 32u] = ent[k];
  }
  const int cl = t >> 2, pc = t & 3;
  if (cl < NB) {
    const unsigned cnt = hist[cl] - 1u;
    u32x4 v = *reinterpret_cast<const u32x4*>(&img[cl * 32 + (cl & 1) * 16 + pc * 4]);
    if (pc == 0) v.x = cnt | (ostart[cl] << 16);
    u32x4* cp = cells + ((size_t)(cl >> 1) * NC + c) * 8 + (cl & 1) * 4 + pc;
    cp[0] = v;
    if (cnt >= 16u) cp[(size_t)((NB + 1) >> 1) * NC * 8] = *reinterpret_cast<const u32x4*>(&img[cl * 32 + ((cl & 1) ^ 1) * 16 + pc * 4]);
  }
  STAMPB(10);
}

static constexpr int kWP   = 8;
static constexpr int kWCap = 248;
static constexpr int kOvf  = 96;
static constexpr int kCtr  = kOvf + 16 * kWCap + 8;
struct __attribute__((aligned(16))) MainLds {
  unsigned list[4096];
  u32x4 wfr[1024];
  u32x4 stage[16 * 512];
};
struct Xd { u32x4 a0, b0, a1, b1; u32x2 t[8]; };

#define WAIT_VM(n) asm volatile("s_waitcnt vmcnt(" #n ")" ::: "memory")
#define WAIT_LGKM0() asm volatile("s_waitcnt lgkmcnt(0)" ::: "memory")
#define LDS_PTR(T, a) ((__attribute__((address_space(3))) T*)(a))
#define GLOAD(dst, ptr, off) asm volatile("global_load_dwordx4 %0, %1, off offset:%2" : "=v"(dst) : "v"(ptr), "n"(off) : "memory")

extern "C" __global__ __launch_bounds__(1024) void k_main(const unsigned short* __restrict__ x,
                                               const u32x4* __restrict__ wfrag,
                                               const u32x4* __restrict__ cells,
                                               const unsigned* __restrict__ covf,
                                               int N, int BN, int NB, int NC, int EC,
                                               unsigned short* __restrict__ out) {
  __shared__ MainLds L;
#define STAMP(i)
  STAMP(0);
  typedef __attribute__((address_space(3))) void* lds_void_t;
  typedef __attribute__((address_space(3))) bf16x4_t* lds_tr_t;
  const int t = threadIdx.x, lane = t & 63;
  const int wave = __builtin_amdgcn_readfirstlane(t >> 6);
  unsigned spf = 0u;
  if (t < 64) {
    unsigned long long pc;
    asm volatile("s_getpc_b64 %0" : "=s"(pc));
    const unsigned char* cpp = reinterpret_cast<const unsigned char*>(pc) + (unsigned)(lane < 36 ? lane : 35) * 128u;
    asm volatile("global_load_dword %0, %1, off" : "=v"(spf) : "v"(cpp) : "memory");
  }
  int b;
  {
    const int qn = NB >> 3, rn = NB & 7, xc = blockIdx.x & 7, ix = blockIdx.x >> 3;
    b = (xc < rn ? xc * (qn + 1) : rn * (qn + 1) + (xc - rn) * qn) + ix;
  }
  const int node0 = b * BN;
  const int ws = (wave * BN + 15) >> 4, we = ((wave + 1) * BN + 15) >> 4;
  const unsigned dummy = kDummy;
  const size_t regB = (size_t)((NB + 1) >> 1) * NC * 8;

  const int c = t >> 2, p = t & 3;
  const int mn = lane & 15, kq = lane >> 4;
  const int lr = lane >> 3;
  const unsigned koff = (unsigned)((lane & 7) ^ lr) * 16u;
  const unsigned sbase = (unsigned)__builtin_amdgcn_readfirstlane((int)(unsigned)(size_t)(lds_void_t)(L.stage + wave * 512));
  const unsigned wbase = (unsigned)__builtin_amdgcn_readfirstlane((int)(unsigned)(size_t)(lds_void_t)(L.wfr));
  u32x4 rs;
  rs.x = (unsigned)(size_t)x; rs.y = (unsigned)((size_t)x >> 32); rs.z = (unsigned)N * (kD * 2); rs.w = 0x00020000u;
  auto dma0 = [&](unsigned la, unsigned o, const u32x4& r) {
    asm volatile("s_mov_b32 m0, %0\n\ts_nop 0\n\tbuffer_load_dwordx4 %1, %2, 0 offen lds" :: "s"(la), "v"(o), "s"(r) : "memory");
  };
  const unsigned obase = (unsigned)(size_t)(lds_void_t)(L.list);
#define PFN 0
#define PFW 16
#define PFO 64
  unsigned pfd[PFN + 1];
  u32x4 ca, cb;
  const u32x4* cp = cells + ((size_t)(b >> 1) * NC + (c < NC ? c : NC - 1)) * 8 + (b & 1) * 4 + p;
  GLOAD(ca, cp, 0);
#define XAPOS 4
  auto xadma = [&]() {
    const unsigned xo = (unsigned)(node0 + ws + lr) * 128u + koff;
    dma0(sbase + 4096u, xo, rs); dma0(sbase + 5120u, xo + 1024u, rs);
  };
  unsigned* ctr = L.list + kCtr;
  {
    const u32x4 dd = {dummy, dummy, dummy, dummy};
    if (t < kCtr / 4) reinterpret_cast<u32x4*>(L.list)[t] = dd;
    if (t < 17) ctr[t] = t < 16 ? obase + (unsigned)(kOvf + t * kWCap) * 4u : 0u;
  }
  __syncthreads();
  STAMP(1);
  {
    u32x4 rw;
    rw.x = (unsigned)(size_t)wfrag; rw.y = (unsigned)((size_t)wfrag >> 32); rw.z = 16384u; rw.w = 0x00020000u;
    dma0(wbase + (unsigned)wave * 1024u, (unsigned)t * 16u, rw);
  }
  asm volatile("s_waitcnt vmcnt(1)" : "+v"(ca), "+v"(spf) :: "memory");
  STAMP(4);
  {
    const unsigned hdr = (unsigned)__builtin_amdgcn_mov_dpp((int)ca.x, 0x00, 0xf, 0xf, true);
    const int cnt = c < NC ? (int)(hdr & 0xffffu) : 0;
    GLOAD(cb, cnt >= 16 ? cp + regB : cp, 0);
    const unsigned kTrash = obase + (unsigned)(kCtr + 20) * 4u;
    constexpr unsigned kInc = 0x10004u, kLim = (unsigned)kWCap << 16;
    auto spill = [&](unsigned e) {
      const unsigned o = atomicAdd(&ctr[16], 1u);
      if (o < (unsigned)kOvf) L.list[o] = e;
    };
    auto bin4 = [&](const u32x4& ev) {
      unsigned r[4];
#pragma unroll
      for (int i = 0; i < 4; ++i) {
        r[i] = kTrash;
        if (ev[i] < dummy) r[i] = atomicAdd(&ctr[ev[i] >> 26], kInc);
      }
      const unsigned m01 = r[0] > r[1] ? r[0] : r[1], m23 = r[2] > r[3] ? r[2] : r[3];
      if (__builtin_expect((m01 > m23 ? m01 : m23) >= kLim, 0)) {
#pragma unroll
        for (int i = 0; i < 4; ++i)
          if (r[i] >= kLim) { spill(ev[i]); r[i] = kTrash; }
      }
#pragma unroll
      for (int i = 0; i < 4; ++i) *LDS_PTR(unsigned, r[i] & 0xffffu) = ev[i];
    };
    if (p == 0) ca.x = dummy;
    if (c < NC) bin4(ca);
    STAMP(8);
    asm volatile("s_waitcnt vmcnt(%1)" : "+v"(cb) : "n"(PFN) : "memory");
    if (cnt >= 16) bin4(cb);
    if (__builtin_expect(cnt > 31, 0)) {
      const unsigned* op = covf + (size_t)c * EC + (hdr >> 16);
      for (int j = p; j < cnt - 31; j += 4) {
        if (*(volatile unsigned*)&ctr[16] > (unsigned)kOvf) break;
        const unsigned e = op[j];
        const unsigned r = atomicAdd(&ctr[e >> 26], kInc);
        if (r < kLim) *LDS_PTR(unsigned, r & 0xffffu) = e;
        else spill(e);
      }
    }
  }
  xadma();
  STAMP(9);
  __syncthreads();
  STAMP(2);
  const int cnt = __builtin_amdgcn_readfirstlane((int)(ctr[wave] >> 16)), novf = __builtin_amdgcn_readfirstlane((int)ctr[16]);
  const bool slow = novf > kOvf;
  const int np = slow ? 0 : (cnt <= kWCap ? (cnt + 31) >> 5 : kWP + ((novf + 31) >> 5));
  const unsigned lbase = obase + (unsigned)(kOvf + wave * kWCap) * 4u;
  auto padr = [&](int j) -> unsigned { return j < kWP ? lbase + (unsigned)j * 128u : obase + (unsigned)(j - kWP) * 128u; };
  const unsigned ar0 = sbase + (unsigned)(mn * 128 + (kq ^ (mn & 7)) * 16);
  const unsigned ar1 = ar0 ^ 64u;
  unsigned at[4];
#pragma unroll
  for (int i = 0; i < 4; ++i)
    at[i] = sbase + (unsigned)((4 * kq + (mn >> 2)) * 128 + (((2 * i + ((mn & 3) >> 1)) ^ (4 * (kq & 1) + (mn >> 2))) * 16) + (mn & 1) * 8);
  const unsigned ln = (unsigned)lr * 16u;
  const unsigned lm = (unsigned)(((mn & 7) * 4 + (mn >> 3)) * 4);
  const unsigned tsel = (unsigned)((wave << 2) | kq);
  u32x2 ident;
  ident.x = (mn == 4 * kq ? 0x3f80u : 0u) | (mn == 4 * kq + 1 ? 0x3f800000u : 0u);
  ident.y = (mn == 4 * kq + 2 ? 0x3f80u : 0u) | (mn == 4 * kq + 3 ? 0x3f800000u : 0u);
  f32x4 acc[4];
#pragma unroll
  for (int i = 0; i < 4; ++i) acc[i] = f32x4{0.f, 0.f, 0.f, 0.f};

  auto dma = [&](unsigned la, unsigned e) {
#define EXP_MASK 0x00ffff80u
    const unsigned o = (e & EXP_MASK) | koff;
    asm volatile("s_mov_b32 m0, %0\n\ts_nop 0\n\tbuffer_load_dwordx4 %1, %2, 0 offen lds" :: "s"(la), "v"(o), "s"(rs) : "memory");
  };
  auto issue = [&](unsigned boff, const u32x4 e) {
    dma(sbase + boff, e.x); dma(sbase + boff + 1024, e.y); dma(sbase + boff + 2048, e.z); dma(sbase + boff + 3072, e.w);
  };
  auto fetch = [&](unsigned boff, Xd& X) {
    X.a0 = *LDS_PTR(u32x4, ar0 + boff); X.b0 = *LDS_PTR(u32x4, ar1 + boff);
    X.a1 = *LDS_PTR(u32x4, ar0 + boff + 2048); X.b1 = *LDS_PTR(u32x4, ar1 + boff + 2048);
#pragma unroll
    for (int nt = 0; nt < 4; ++nt) {
      X.t[2 * nt]     = __builtin_bit_cast(u32x2, __builtin_amdgcn_ds_read_tr16_b64_v4i16((lds_tr_t)(at[nt] + boff)));
      X.t[2 * nt + 1] = __builtin_bit_cast(u32x2, __builtin_amdgcn_ds_read_tr16_b64_v4i16((lds_tr_t)(at[nt] + boff + 2048)));
    }
  };
  u32x4 q0, q1;
  auto logits = [&](const Xd& X, f32x4& l0, f32x4& l1) {
    const f32x4 z = {0.f, 0.f, 0.f, 0.f};
    l0 = mfma32(q1, X.b0, mfma32(q0, X.a0, z));
    l1 = mfma32(q1, X.b1, mfma32(q0, X.a1, z));
  };
  auto rest = [&](const Xd& X, const f32x4& l0, const f32x4& l1, unsigned e0, unsigned e1) {
    const f32x4 z = {0.f, 0.f, 0.f, 0.f};
    const float u0 = (e0 & 16u) ? l0[1] : l0[0], w0 = (e0 & 16u) ? l0[3] : l0[2];
    const float u1 = (e1 & 16u) ? l1[1] : l1[0], w1 = (e1 & 16u) ? l1[3] : l1[2];
    const float d0 = (e0 & 32u) ? w0 : u0, d1 = (e1 & 32u) ? w1 : u1;
    const unsigned pk = cvtpk(d0, d1);
    f32x2 lg = {__builtin_bit_cast(float, pk << 16), __builtin_bit_cast(float, pk & 0xffff0000u)};
    const f32x2 cz = {-0.125f * 1.4426950408889634f, -0.125f * 1.4426950408889634f};
    lg = lg * cz;
    f32x2 ex = {__builtin_amdgcn_exp2f(lg.x), __builtin_amdgcn_exp2f(lg.y)};
    const f32x2 one = {1.0f, 1.0f};
    ex = ex + one;
    const float r0 = __builtin_amdgcn_rcpf(ex.x), r1 = __builtin_amdgcn_rcpf(ex.y);
    const bool o0 = (e0 >> 24) == tsel, o1 = (e1 >> 24) == tsel;
    const unsigned long long h0 = (unsigned long long)cvtpk(o0 ? r0 : 0.f, 0.f) << (e0 & 63u);
    const unsigned long long h1 = (unsigned long long)cvtpk(o1 ? r1 : 0.f, 0.f) << (e1 & 63u);
    const f32x4 t0 = mfma16(__builtin_bit_cast(u32x2, h0), ident, z);
    const f32x4 t1 = mfma16(__builtin_bit_cast(u32x2, h1), ident, z);
    const u32x4 al = {cvtpk(t0[0], t0[1]), cvtpk(t0[2], t0[3]), cvtpk(t1[0], t1[1]), cvtpk(t1[2], t1[3])};
#pragma unroll
    for (int nt = 0; nt < 4; ++nt) {
      const u32x4 xt = {X.t[2 * nt].x, X.t[2 * nt].y, X.t[2 * nt + 1].x, X.t[2 * nt + 1].y};
      acc[nt] = mfma32(xt, al, acc[nt]);
    }
  };
  auto compute = [&](const Xd& X, unsigned e0, unsigned e1) {
    f32x4 l0, l1;
    logits(X, l0, l1);
    rest(X, l0, l1, e0, e1);
  };
  auto body = [&](unsigned bo, int j) {
    const u32x4 nx = *LDS_PTR(u32x4, padr(j + 1) + ln);
    const unsigned pa = padr(j) + lm;
    const unsigned m0 = *LDS_PTR(unsigned, pa), m1 = *LDS_PTR(unsigned, pa + 8u);
    Xd X; f32x4 l0, l1;
    WAIT_VM(0);
    fetch(bo, X);
    __builtin_amdgcn_sched_barrier(0);
    logits(X, l0, l1);
    WAIT_LGKM0();
    issue(bo, nx);
    __builtin_amdgcn_sched_barrier(0);
    rest(X, l0, l1, m0, m1);
  };
  STAMP(10);
  issue(0u, *LDS_PTR(u32x4, lbase + ln));
  WAIT_VM(4);
#pragma unroll
  for (int i = 0; i < PFN; ++i) asm volatile("" :: "v"(pfd[i]));
  const u32x4 xa0 = *LDS_PTR(u32x4, ar0 + 4096u), xa1 = *LDS_PTR(u32x4, ar1 + 4096u);
  {
    u32x4 wf[8];
#pragma unroll
    for (int i = 0; i < 8; ++i) wf[i] = L.wfr[i * 64 + lane];
    const f32x4 z = {0.f, 0.f, 0.f, 0.f};
    f32x4 cq[4];
#pragma unroll
    for (int tt = 0; tt < 4; ++tt) cq[tt] = mfma32(wf[2 * tt + 1], xa1, mfma32(wf[2 * tt], xa0, z));
    q0.x = cvtpk(cq[0][0], cq[0][1]); q0.y = cvtpk(cq[0][2], cq[0][3]);
    q0.z = cvtpk(cq[1][0], cq[1][1]); q0.w = cvtpk(cq[1][2], cq[1][3]);
    q1.x = cvtpk(cq[2][0], cq[2][1]); q1.y = cvtpk(cq[2][2], cq[2][3]);
    q1.z = cvtpk(cq[3][0], cq[3][1]); q1.w = cvtpk(cq[3][2], cq[3][3]);
  }
  STAMP(3);
  int pp = 0;
  for (; pp + 1 < np; ++pp) {
    body(0u, pp);
  }
  if (pp < np) {
    const unsigned pa = padr(pp) + lm;
    const unsigned m0 = *LDS_PTR(unsigned, pa), m1 = *LDS_PTR(unsigned, pa + 8u);
    Xd X;
    WAIT_VM(0);
    fetch(0u, X);
    compute(X, m0, m1);
  }
  STAMP(5);
  if (__builtin_expect(slow, 0)) {
    const unsigned* cw = reinterpret_cast<const unsigned*>(cells);
    const unsigned ovo = (unsigned)(covf - cw), rbw = (unsigned)regB * 4u;
    for (int sg = 0; sg < 2 * NC; ++sg) {
      const unsigned co = ((unsigned)(b >> 1) * (unsigned)NC + (unsigned)(sg >> 1)) * 32u + (unsigned)(b & 1) * 16u;
      const unsigned hdr = (unsigned)__builtin_amdgcn_readfirstlane((int)cw[co]);
      const int cn = (int)(hdr & 0xffffu);
      const bool ov = (sg & 1) != 0;
      const unsigned po = ovo + (unsigned)(sg >> 1) * (unsigned)EC + (hdr >> 16);
      const int len = ov ? cn - 31 : (cn > 0 ? 32 : 0);
      for (int j = 0; j < len; j += 32) {
        auto ent = [&](int i) -> unsigned {
          const int jj = (i & 16) + 4 * (i & 3) + ((i >> 2) & 3);
          const bool ok = ov ? (j + i < len) : (jj >= 1 && jj <= cn);
          const unsigned q = ov ? po + (unsigned)(j + i) : co + (unsigned)(i & 15) + (unsigned)(i >> 4) * rbw;
          return ok ? cw[q] : dummy;
        };
        u32x4 nx;
        nx.x = ent(4 * lr); nx.y = ent(4 * lr + 1); nx.z = ent(4 * lr + 2); nx.w = ent(4 * lr + 3);
        const unsigned m0 = ent((mn & 7) * 4 + (mn >> 3)), m1 = ent((mn & 7) * 4 + (mn >> 3) + 2);
        Xd X;
        WAIT_VM(0);
        WAIT_LGKM0();
        issue(0u, nx);
        WAIT_VM(0);
        fetch(0u, X);
        compute(X, m0, m1);
      }
    }
  }
  WAIT_VM(0);
  STAMP(6);
  {
    u32x4 wf[8];
#pragma unroll
    for (int i = 0; i < 8; ++i) wf[i] = L.wfr[512 + i * 64 + lane];
    u32x4 bh[2], bl[2];
#pragma unroll
    for (int h = 0; h < 2; ++h) {
#pragma unroll
      for (int k = 0; k < 4; ++k) {
        const float s0 = acc[2 * h + (k >> 1)][2 * (k & 1)], s1 = acc[2 * h + (k >> 1)][2 * (k & 1) + 1];
        const unsigned hp = cvtpk(s0, s1);
        const float f0 = __builtin_bit_cast(float, hp << 16), f1 = __builtin_bit_cast(float, hp & 0xffff0000u);
        bh[h][k] = hp; bl[h][k] = cvtpk(s0 - f0, s1 - f1);
      }
    }
    f32x4 co[4];
#pragma unroll
    for (int tt = 0; tt < 4; ++tt) {
      f32x4 z = {0.f, 0.f, 0.f, 0.f};
      z = mfma32(wf[2 * tt], bl[0], z); z = mfma32(wf[2 * tt + 1], bl[1], z);
      z = mfma32(wf[2 * tt], bh[0], z); z = mfma32(wf[2 * tt + 1], bh[1], z);
      co[tt] = z;
    }
    u32x4 p0, p1;
    p0.x = cvtpk(co[0][0], co[0][1]); p0.y = cvtpk(co[0][2], co[0][3]);
    p0.z = cvtpk(co[1][0], co[1][1]); p0.w = cvtpk(co[1][2], co[1][3]);
    p1.x = cvtpk(co[2][0], co[2][1]); p1.y = cvtpk(co[2][2], co[2][3]);
    p1.z = cvtpk(co[3][0], co[3][1]); p1.w = cvtpk(co[3][2], co[3][3]);
    const unsigned ow = sbase + (unsigned)(mn * 128 + ((2 * kq) ^ (mn & 7)) * 16);
    *LDS_PTR(u32x4, ow) = p0; *LDS_PTR(u32x4, ow ^ 16u) = p1;
    const u32x4 r0 = *LDS_PTR(u32x4, sbase + (unsigned)lane * 16u), r1 = *LDS_PTR(u32x4, sbase + 1024u + (unsigned)lane * 16u);
    int lim = N - node0; lim = lim < we ? lim : we;
    unsigned char* op = reinterpret_cast<unsigned char*>(out) + (size_t)(node0 + ws + lr) * 128u + koff;
    if (ws + lr < lim) asm volatile("global_store_dwordx4 %0, %1, off sc1\n\ts_nop 2" :: "v"(op), "v"(r0) : "memory");
    if (ws + lr + 8 < lim) asm volatile("global_store_dwordx4 %0, %1, off offset:1024 sc1\n\ts_nop 2" :: "v"(op), "v"(r1) : "memory");
  }
  STAMP(7);
}

__device__ __forceinline__ float bf2f(unsigned short v) { return __builtin_bit_cast(float, (unsigned)v << 16); }

__global__ __launch_bounds__(256) void f_prep(const unsigned short* __restrict__ x, const unsigned short* __restrict__ wq,
                                              int N, unsigned short* __restrict__ Q, float* __restrict__ S) {
  const size_t i = (size_t)blockIdx.x * 256 + threadIdx.x;
  if (i >= (size_t)N * kD) return;
  const size_t n = i >> 6; const int f = (int)(i & 63);
  float a = 0.f;
  for (int k = 0; k < kD; ++k) a = fmaf(bf2f(x[n * kD + k]), bf2f(wq[k * kD + f]), a);
  Q[i] = (unsigned short)(cvtpk(a, a) & 0xffffu);
  S[i] = 0.f;
}
__global__ __launch_bounds__(256) void f_edge(const unsigned short* __restrict__ x, const unsigned short* __restrict__ Q,
                                              const int* __restrict__ src, const int* __restrict__ dst, int E,
                                              float* __restrict__ S) {
  const size_t gid = ((size_t)blockIdx.x * 256 + threadIdx.x) >> 3;
  const int p = threadIdx.x & 7;
  const bool ok = gid < (size_t)E;
  const size_t e = ok ? gid : 0;
  const int s = src[e], d = dst[e];
  const u32x4 xr = *reinterpret_cast<const u32x4*>(x + (size_t)s * kD + p * 8);
  const u32x4 qv = *reinterpret_cast<const u32x4*>(Q + (size_t)d * kD + p * 8);
  float dd = dot2(xr.x, qv.x, 0.0f);
  dd = dot2(xr.y, qv.y, dd); dd = dot2(xr.z, qv.z, dd); dd = dot2(xr.w, qv.w, dd);
  dd = group8_sum(dd);
  const unsigned al = alpha_pk(dd), ah = al << 16;
  if (ok) {
    float* o = S + (size_t)d * kD + p * 8;
    atomicAdd(o + 0, dot2(xr.x, al, 0.f)); atomicAdd(o + 1, dot2(xr.x, ah, 0.f));
    atomicAdd(o + 2, dot2(xr.y, al, 0.f)); atomicAdd(o + 3, dot2(xr.y, ah, 0.f));
    atomicAdd(o + 4, dot2(xr.z, al, 0.f)); atomicAdd(o + 5, dot2(xr.z, ah, 0.f));
    atomicAdd(o + 6, dot2(xr.w, al, 0.f)); atomicAdd(o + 7, dot2(xr.w, ah, 0.f));
  }
}
__global__ __launch_bounds__(256) void f_out(const float* __restrict__ S, const unsigned short* __restrict__ wv,
                                             int N, unsigned short* __restrict__ out) {
  const size_t i = (size_t)blockIdx.x * 256 + threadIdx.x;
  if (i >= (size_t)N * kD) return;
  const size_t n = i >> 6; const int f = (int)(i & 63);
  float a = 0.f;
  for (int k = 0; k < kD; ++k) a = fmaf(S[n * kD + k], bf2f(wv[k * kD + f]), a);
  out[i] = (unsigned short)(cvtpk(a, a) & 0xffffu);
}

extern "C" void kernel_launch(void* const* d_in, const int* in_sizes, int n_in,
                              void* d_out, int out_size, void* d_ws, size_t ws_size,
                              hipStream_t stream) {
  const unsigned short* x  = (const unsigned short*)d_in[0];
  const int* src = (const int*)d_in[1];
  const int* dst = (const int*)d_in[2];
  const unsigned short* wq = (const unsigned short*)d_in[3];
  const unsigned short* wv = (const unsigned short*)d_in[4];
  unsigned short* out = (unsigned short*)d_out;
  const int N = in_sizes[0] / kD;
  const int E = in_sizes[1];
  if (N <= 0) return;

  int BN = (N + 255) / 256; if (BN < 2) BN = 2;
  const int NB = (N + BN - 1) / BN;
  int NC = kMaxNC;
  int EC = (E + NC - 1) / NC; EC = (EC + 3) & ~3; if (EC < 4) EC = 4;
  NC = E > 0 ? (E + EC - 1) / EC : 1;
  const size_t w_cells = (size_t)((NB + 1) >> 1) * NC * 64, w_covf = ((size_t)NC * EC + 63) & ~(size_t)63;
  const bool fast = (BN <= kMaxBN) && (NB <= kMaxNB) && (EC <= kMaxEC) && (E >= 4) &&
                    ((w_cells + w_covf + 4096) * 4 <= ws_size);
  if (fast) {
    unsigned* cells = (unsigned*)d_ws;
    unsigned* covf = cells + w_cells;
    u32x4* wfrag = (u32x4*)(covf + w_covf);
    const unsigned magic = (unsigned)((0x100000000ull / (unsigned)BN) + 1ull);
    k_bucket<<<NC, 1024, 0, stream>>>(src, dst, wq, wv, E, EC, BN, magic, NB, NC, (u32x4*)cells, covf, wfrag, x, N);
    k_main<<<NB, 1024, 0, stream>>>(x, wfrag, (const u32x4*)cells, covf, N, BN, NB, NC, EC, out);
  } else {
    float* S = (float*)d_ws;
    unsigned short* Q = (unsigned short*)(S + (size_t)N * kD);
    const unsigned gn = (unsigned)(((size_t)N * kD + 255) / 256);
    f_prep<<<gn, 256, 0, stream>>>(x, wq, N, Q, S);
    if (E > 0) f_edge<<<(unsigned)(((size_t)E * 8 + 255) / 256), 256, 0, stream>>>(x, Q, src, dst, E, S);
    f_out<<<gn, 256, 0, stream>>>(S, wv, N, out);
  }
}
```

```cpp
#define CPN 84
#define P1 3
#define P2 1
#include <hip/hip_runtime.h>
#include <hip/hip_bf16.h>
#include <stdint.h>

typedef __attribute__((ext_vector_type(2))) __bf16 bf16x2_t;
typedef __attribute__((ext_vector_type(8))) short  bf16x8_t;
typedef __attribute__((ext_vector_type(4))) short  bf16x4_t;
typedef __attribute__((ext_vector_type(4))) unsigned u32x4;
typedef __attribute__((ext_vector_type(2))) unsigned u32x2;
typedef __attribute__((ext_vector_type(4))) int      i32x4;
typedef __attribute__((ext_vector_type(4))) float    f32x4;
typedef __attribute__((ext_vector_type(2))) float    f32x2;

#define FEAT(h, kq, j) (32 * (h) + 8 * (kq) + (j))
#define DEPTH 3
static constexpr int kD        = 64;
static constexpr int kMaxBN    = 256;
static constexpr int kMaxNB    = 256;
static constexpr int kMaxEC    = 4096;
static constexpr int kMaxNC    = 256;
static constexpr unsigned kDummy = 0xffffffffu;
#define KNONE 0x3f000000u
static constexpr unsigned kNone  = KNONE;

__device__ __forceinline__ float dot2(unsigned a, unsigned b, float acc) {
  return __builtin_amdgcn_fdot2_f32_bf16(__builtin_bit_cast(bf16x2_t, a), __builtin_bit_cast(bf16x2_t, b), acc, false);
}
__device__ __forceinline__ unsigned cvtpk(float lo, float hi) {
  const f32x2 v = {lo, hi};
  return __builtin_bit_cast(unsigned, __builtin_convertvector(v, bf16x2_t));
}
template <int CTRL> __device__ __forceinline__ float dpp(float x) {
  return __builtin_bit_cast(float, __builtin_amdgcn_mov_dpp(__builtin_bit_cast(int, x), CTRL, 0xf, 0xf, true));
}
__device__ __forceinline__ float group8_sum(float d) {
  d += dpp<0xB1>(d);
  d += dpp<0x4E>(d);
  d += dpp<0x141>(d);
  return d;
}
__device__ __forceinline__ unsigned wave_incl_scan(unsigned v) {
  v += (unsigned)__builtin_amdgcn_update_dpp(0, (int)v, 0x111, 0xf, 0xf, true);
  v += (unsigned)__builtin_amdgcn_update_dpp(0, (int)v, 0x112, 0xf, 0xf, true);
  v += (unsigned)__builtin_amdgcn_update_dpp(0, (int)v, 0x114, 0xf, 0xf, true);
  v += (unsigned)__builtin_amdgcn_update_dpp(0, (int)v, 0x118, 0xf, 0xf, true);
  v += (unsigned)__builtin_amdgcn_update_dpp(0, (int)v, 0x142, 0xa, 0xf, false);
  v += (unsigned)__builtin_amdgcn_update_dpp(0, (int)v, 0x143, 0xc, 0xf, false);
  return v;
}
__device__ __forceinline__ unsigned alpha_pk(float d) {
  const float lg = __builtin_bit_cast(float, cvtpk(d, d) << 16);
  const float z  = lg * (-0.125f * 1.4426950408889634f);
  const float ex = __builtin_amdgcn_exp2f(z);
  const float a  = __builtin_amdgcn_rcpf(1.0f + ex);
  return cvtpk(a, 0.0f);
}
__device__ __forceinline__ unsigned alpha2(float d0, float d1) {
  const unsigned pk = cvtpk(d0, d1);
  f32x2 lg = {__builtin_bit_cast(float, pk << 16), __builtin_bit_cast(float, pk & 0xffff0000u)};
  const f32x2 cz = {-0.125f * 1.4426950408889634f, -0.125f * 1.4426950408889634f};
  lg = lg * cz;
  f32x2 ex = {__builtin_amdgcn_exp2f(lg.x), __builtin_amdgcn_exp2f(lg.y)};
  const f32x2 one = {1.0f, 1.0f};
  ex = ex + one;
  return cvtpk(__builtin_amdgcn_rcpf(ex.x), __builtin_amdgcn_rcpf(ex.y));
}
__device__ __forceinline__ float sel4(const f32x4& l, unsigned e) {
  unsigned ma, mb, u, w, d;
  ma = (unsigned)__builtin_amdgcn_sbfe((int)e, 4u, 1u); mb = (unsigned)__builtin_amdgcn_sbfe((int)e, 5u, 1u);
  asm("" : "+v"(ma), "+v"(mb));
  const u32x4 v = __builtin_bit_cast(u32x4, l);
  u = (ma & v.y) | (~ma & v.x);
  w = (ma & v.w) | (~ma & v.z);
  d = (mb & w) | (~mb & u);
  return __builtin_bit_cast(float, d);
}
__device__ __forceinline__ f32x4 mfma32(const u32x4& a, const u32x4& b, const f32x4& c) {
  return __builtin_amdgcn_mfma_f32_16x16x32_bf16(__builtin_bit_cast(bf16x8_t, a), __builtin_bit_cast(bf16x8_t, b), c, 0, 0, 0);
}
__device__ __forceinline__ f32x4 mfma16(const u32x2& a, const u32x2& b, const f32x4& c) {
  return __builtin_amdgcn_mfma_f32_16x16x16bf16_1k(__builtin_bit_cast(bf16x4_t, a), __builtin_bit_cast(bf16x4_t, b), c, 0, 0, 0);
}

static constexpr int kSub  = 128;
static constexpr int kWCap = 448;
struct __attribute__((aligned(16))) MainLds {
  unsigned ctr[256];
  unsigned hist[kMaxNB];
  unsigned tab[kMaxBN];
  unsigned miss[kMaxNC];
  u32x4 wfr[1024];
  u32x4 stage[16 * 256];
  u32x4 own[16 * 128];
  u32x4 list[16 * 128];
};
struct Xd { u32x4 a0, b0, a1, b1; u32x2 t[8]; };

#define WAIT_VM(n) asm volatile("s_waitcnt vmcnt(" #n ")" ::: "memory")
#define WAIT_LGKM0() asm volatile("s_waitcnt lgkmcnt(0)" ::: "memory")
#define LDS_PTR(T, a) ((__attribute__((address_space(3))) T*)(a))
#define GLOADC(dst, ptr) asm volatile("global_load_dwordx4 %0, %1, off " LPOL : "=v"(dst) : "v"(ptr) : "memory")
#define GLOADW(dst, ptr) asm volatile("global_load_dwordx4 %0, %1, off " LPOL "\n\ts_waitcnt vmcnt(0)" : "=v"(dst) : "v"(ptr) : "memory")
#define SPOL "sc1"
#define LPOL "sc1"
#define GSTOREC(ptr, v) asm volatile("global_store_dwordx4 %0, %1, off " SPOL "\n\ts_nop 2" :: "v"(ptr), "v"(v) : "memory")
#define WPOS 1
#define XPOS 2
#define TMO 4000u

extern "C" __device__ unsigned long long amd_dispatch_id() __asm("llvm.amdgcn.dispatch.id");
__device__ __forceinline__ unsigned quad_xor(const u32x4& v) {
  unsigned s = v.x ^ v.y ^ v.z ^ v.w;
  s ^= (unsigned)__builtin_amdgcn_mov_dpp((int)s, 0xB1, 0xf, 0xf, true);
  s ^= (unsigned)__builtin_amdgcn_mov_dpp((int)s, 0x4E, 0xf, 0xf, true);
  return s;
}

extern "C" __global__ __launch_bounds__(1024) void k_main(const unsigned short* __restrict__ x,
                                               const int* __restrict__ src, const int* __restrict__ dst,
                                               const unsigned short* __restrict__ wq, const unsigned short* __restrict__ wv,
                                               u32x4* __restrict__ cells, unsigned short* __restrict__ out,
                                               int E, int EC, int NC, int N, int BN, unsigned magic, int NB,
                                               unsigned cmul, unsigned rmul, unsigned zp) {
  __shared__ MainLds L;
#define STAMP(i)
  STAMP(0);
  typedef __attribute__((address_space(3))) void* lds_void_t;
  typedef __attribute__((address_space(3))) bf16x4_t* lds_tr_t;
  const int t = threadIdx.x;
  unsigned spf = 0u;
  if (t < 128) {
    unsigned long long pc;
    asm volatile("s_getpc_b64 %0" : "=s"(pc));
    const unsigned char* cpp = reinterpret_cast<const unsigned char*>(pc) + (unsigned)(t < CPN ? t : CPN - 1) * 128u;
    asm volatile("global_load_dword %0, %1, off" : "=v"(spf) : "v"(cpp) : "memory");
  }
  const int ch = blockIdx.x;
  const int base = ch * EC;
  int n = E - base; n = n < EC ? n : EC; n = (n < 0 || ch >= NC) ? 0 : n;
  const int i0 = t * 4;
  const bool full = i0 + 3 < n;
  i32x4 d4, s4;
  {
    const int* dp = dst + (full ? base + i0 : 0);
    const int* sp = src + (full ? base + i0 : 0);
    asm volatile("global_load_dwordx4 %0, %1, off" : "=v"(d4) : "v"(dp) : "memory");
    asm volatile("global_load_dwordx4 %0, %1, off" : "=v"(s4) : "v"(sp) : "memory");
  }
  u32x2 sa, sb;
  asm volatile("global_load_dwordx2 %0, %1, %2" : "=v"(sa) : "v"(0u), "s"(dst) : "memory");
  asm volatile("global_load_dwordx2 %0, %1, %2" : "=v"(sb) : "v"(0u), "s"(src) : "memory");
  unsigned* ctr = L.ctr;
  unsigned* img = reinterpret_cast<unsigned*>(L.stage);
  if (t < kMaxNB) L.hist[t] = 4u + (((unsigned)t * rmul) & 0x3800000u);
  if (t >= 256 && t < 256 + 131) {
    const unsigned i = (unsigned)t - 256u;
    ctr[i] = i < 128u ? ((unsigned)(size_t)(lds_void_t)(L.stage) >> 2) + (i >> 3) * 1024u + (i & 7u) * (unsigned)kSub : 0u;
  }
  if (t >= 512 && t < 512 + kMaxBN) {
    const unsigned dl = (unsigned)t - 512u;
    const unsigned z0 = zp & 255u, z1 = (zp >> 8) & 255u, z2 = (zp >> 16) & 255u, z3 = zp >> 24;
    const unsigned t1 = 4u * z0, t2 = t1 + 4u * z1, t3 = t2 + 4u * z2;
    const unsigned g = (dl >= t1) + (dl >= t2) + (dl >= t3);
    const unsigned r = dl - (dl >= t3 ? t3 : dl >= t2 ? t2 : dl >= t1 ? t1 : 0u);
    const unsigned z = dl >= t3 ? z3 : dl >= t2 ? z2 : dl >= t1 ? z1 : z0;
    const unsigned i = (r >= z) + (r >= 2u * z) + (r >= 3u * z);
    const unsigned pos = (r - i * z) & 15u;
    L.tab[dl] = ((pos >> 2) << 30) | ((g * 4u + i) << 26) | ((pos & 3u) << 4);
  }
  {
    const u32x4 dd = {kDummy, kDummy, kDummy, kDummy};
    L.stage[(t >> 2) * 12 + (t & 3)] = dd; L.stage[(t >> 2) * 12 + 4 + (t & 3)] = dd;
  }
  asm volatile("s_waitcnt lgkmcnt(0)\n\ts_barrier" ::: "memory");
  STAMP(1);
  unsigned nonce;
  {
    const unsigned long long di = amd_dispatch_id();
    const unsigned long long dq = (unsigned long long)(size_t)__builtin_amdgcn_dispatch_ptr();
    nonce = (((unsigned)di * 0x9E3779B1u) ^ ((unsigned)(dq >> 6) * 0x85EBCA77u) ^ (unsigned)(di >> 32) ^ (unsigned)(dq >> 38));
    asm volatile("" : "+s"(nonce));
  }
  const unsigned dummy = kDummy;
  const size_t regB = (size_t)((NB + 1) >> 1) * NC * 8;
  const int c = t >> 2, p = t & 3;
  u32x4* pw = cells + ((size_t)(c >> 1) * NC + ch) * 8 + (c & 1) * 4 + p;
  asm volatile("" : "+v"(pw));
  asm volatile("s_waitcnt vmcnt(0)" : "+v"(d4), "+v"(s4), "+v"(spf), "+v"(sa), "+v"(sb) :: "memory");
  STAMP(4);
  const unsigned tabb = (unsigned)(size_t)(lds_void_t)(L.tab), imgb = (unsigned)(size_t)(lds_void_t)(L.stage);
  const int nbn = -BN;
  auto bucket = [&](unsigned d) -> unsigned {
    unsigned r;
    asm("v_mul_hi_u32_u24 %0, %1, %2" : "=v"(r) : "s"(magic), "v"(d << 8));
    return r;
  };
  auto tabv = [&](unsigned d, unsigned bkt) -> unsigned {
    unsigned dl;
    asm("v_mad_i32_i24 %0, %1, %2, %3" : "=v"(dl) : "v"(bkt), "s"(nbn), "v"(d));
    return *LDS_PTR(unsigned, tabb + (dl << 2));
  };
  auto mkent = [&](unsigned tv, unsigned sidx, unsigned rk) -> unsigned {
    unsigned cl;
    asm("v_mad_u32_u24 %0, %1, %2, %3" : "=v"(cl) : "v"(sidx), "s"(cmul), "v"(rk));
    return ((cl & 0x3800000u) | tv) | (sidx << 7);
  };
  auto put = [&](unsigned bkt, unsigned rk, unsigned e) {
    unsigned r = rk & 0xffffu, a;
    r = r < 124u ? r : 124u;
    asm("v_mad_u32_u24 %0, %1, %2, %3" : "=v"(a) : "v"(bkt), "s"(192u), "v"(r));
    *LDS_PTR(unsigned, imgb + a) = e;
  };
  if (__builtin_expect(full, 1)) {
    unsigned bk[4], rk[4], tv[4];
#pragma unroll
    for (int k = 0; k < 4; ++k) bk[k] = bucket((unsigned)d4[k]);
#pragma unroll
    for (int k = 0; k < 4; ++k) rk[k] = atomicAdd(&L.hist[bk[k]], 4u);
#pragma unroll
    for (int k = 0; k < 4; ++k) tv[k] = tabv((unsigned)d4[k], bk[k]);
    __builtin_amdgcn_sched_barrier(0);
#pragma unroll
    for (int k = 0; k < 4; ++k) put(bk[k], rk[k], mkent(tv[k], (unsigned)s4[k], rk[k]));
  }
  asm volatile("" ::: "memory");
  if (__builtin_expect(!full && i0 < n, 0)) {
    for (int k = 0; k < 3; ++k) {
      if (i0 + k < n) {
        const unsigned d = (unsigned)dst[base + i0 + k];
        const unsigned bk = bucket(d);
        const unsigned rk = atomicAdd(&L.hist[bk], 4u);
        put(bk, rk, mkent(tabv(d, bk), (unsigned)src[base + i0 + k], rk));
      }
    }
  }
  {
    const unsigned hb = sb.x + (sb.y << 16);
    nonce += sa.x + (sa.y << 16) + __builtin_amdgcn_alignbit(hb, hb, 11u);
    nonce = (nonce & ~3u) | 0x80000000u;
  }
  asm volatile("s_waitcnt lgkmcnt(0)\n\ts_barrier" ::: "memory");
  STAMP(8);
  {
    unsigned cnt = L.hist[c];
    u32x4 v = L.stage[c * 12 + p];
    u32x4 u = L.stage[c * 12 + 4 + p];
    asm volatile("" : "+v"(cnt));
    cnt &= 0xffffu;
    const unsigned hv = ~quad_xor(v) ^ nonce ^ (cnt > 124u ? 2u : cnt > 64u ? 1u : 0u), hu = ~quad_xor(u) ^ nonce;
    if (p == 0) v.x = hv;
    if (p == 3) u.w = hu;
    if (c < NB && ch < NC) {
      GSTOREC(pw, v);
      if (cnt - 68u < 60u) GSTOREC(pw + regB, u);
    }
  }
  if ((int)blockIdx.x >= NB) return;
  int lane = t;
  asm volatile("" : "+v"(lane));
  const int c2 = lane >> 2;
  const int wave = __builtin_amdgcn_readfirstlane(lane >> 6);
  int bid = blockIdx.x;
  asm volatile("" : "+s"(bid));
  const unsigned sbase = (unsigned)(size_t)(lds_void_t)(L.stage) + (unsigned)wave * 4096u;
  const unsigned wbase = (unsigned)(size_t)(lds_void_t)(L.wfr);
  auto dma0 = [&](unsigned la, unsigned o, const u32x4& r) {
    asm volatile("s_mov_b32 m0, %0\n\ts_nop 0\n\tbuffer_load_dwordx4 %1, %2, 0 offen lds" :: "s"(la), "v"(o), "s"(r) : "memory");
  };
  int b;
  {
    const int qn = NB >> 3, rn = NB & 7, xc = bid & 7, ix = bid >> 3;
    b = (xc < rn ? xc * (qn + 1) : rn * (qn + 1) + (xc - rn) * qn) + ix;
  }
  const int node0 = b * BN;
  int ws, we;
  {
    const int g = wave >> 2;
    const int z = (int)((zp >> (8 * g)) & 255u);
    const unsigned lo = zp & ((1u << (8 * g)) - 1u);
    ws = 4 * (int)((lo & 255u) + ((lo >> 8) & 255u) + (lo >> 16)) + (wave & 3) * z;
    we = ws + z;
    ws = ws < BN ? ws : BN; we = we < BN ? we : BN;
  }
  u32x4 rs;
  rs.x = (unsigned)(size_t)x; rs.y = (unsigned)((size_t)x >> 32); rs.z = (unsigned)N * (kD * 2); rs.w = 0x00020000u;
  const unsigned obase = (unsigned)(size_t)(lds_void_t)(L.own) + (unsigned)wave * 2048u;
  const unsigned lbase = (unsigned)(size_t)(lds_void_t)(L.list) + (unsigned)wave * 2048u;
  lane &= 63;
  const int mn = lane & 15, kq = lane >> 4;
  const int lr = lane >> 3;
  const unsigned koff = (unsigned)((lane & 7) ^ lr) * 16u;
  unsigned ar0, ar1, at[4], ln, lm, tsel, sl;
  u32x2 ident;
  ar0 = sbase + (unsigned)(mn * 128 + (kq ^ (mn & 7)) * 16);
  ar1 = ar0 ^ 64u;
#pragma unroll
  for (int i = 0; i < 4; ++i)
    at[i] = sbase + (unsigned)((4 * kq + (mn >> 2)) * 128 + (((2 * i + ((mn & 3) >> 1)) ^ (4 * (kq & 1) + (mn >> 2))) * 16) + (mn & 1) * 8);
  ln = lbase + (unsigned)lr * 16u;
  lm = lbase + (unsigned)(((mn & 7) * 4 + (mn >> 3)) * 4);
  tsel = (unsigned)(kq << 6);
  ident.x = (mn == 4 * kq ? 0x3f80u : 0u) | (mn == 4 * kq + 1 ? 0x3f800000u : 0u);
  ident.y = (mn == 4 * kq + 2 ? 0x3f80u : 0u) | (mn == 4 * kq + 3 ? 0x3f800000u : 0u);
  sl = sbase + (unsigned)lane * 4u;
  const u32x4* cp = cells + ((size_t)(b >> 1) * NC + (c2 < NC ? c2 : NC - 1)) * 8 + (b & 1) * 4 + (lane & 3);
  asm volatile("" : "+v"(ar0), "+v"(ar1), "+v"(at[0]), "+v"(at[1]), "+v"(at[2]), "+v"(at[3]), "+v"(ln), "+v"(lm), "+v"(tsel), "+v"(ident), "+v"(sl));
  asm volatile("" : "+v"(cp));
  unsigned wofs, xo0, xo1, wdst = wbase + (unsigned)wave * 1024u;
  u32x4 rw;
  {
    const bool isq = wave < 8;
    const unsigned short* w = isq ? wq : wv;
    const int g = wave & 7, blk = g >> 1, hf = g & 1, r = mn >> 2, pc = mn & 3;
    const int k = isq ? 32 * (blk >> 1) + 8 * kq + 4 * (blk & 1) + r : 16 * blk + 4 * kq + r;
    const int cg = isq ? 4 * hf + pc : 2 * pc + hf;
    rw.x = (unsigned)(size_t)w; rw.y = (unsigned)((size_t)w >> 32); rw.z = 8192u; rw.w = 0x00020000u;
    wofs = (unsigned)(k * 128 + cg * 16);
    xo0 = (int)blockIdx.x < NB ? (unsigned)(node0 + ws + lr) * 128u + koff : 0xfffff000u;
    xo1 = ws + lr + 8 < we ? xo0 + 1024u : 0xffffff00u;
  }
  asm volatile("" : "+v"(wofs), "+v"(xo0), "+v"(xo1), "+s"(rw), "+s"(rs), "+s"(wdst), "+s"(out), "+s"(ws), "+s"(we));
  auto wdma = [&]() { dma0(wdst, wofs, rw); };
  auto xdma = [&]() { dma0(obase, xo0, rs); dma0(obase + 1024u, xo1, rs); };
  u32x4 ca, cb;
  asm volatile("s_waitcnt vmcnt(0) lgkmcnt(0)\n\ts_barrier" ::: "memory");
  STAMP(9);
  GLOADC(ca, cp);
  unsigned t0 = (unsigned)__builtin_amdgcn_s_memrealtime();
  asm volatile("" : "+s"(t0));
  u32x4 q0, q1;
  const unsigned wl = wbase + (unsigned)lane * 16u;
  auto wfrag = [&](int m, int tt, int sx) -> u32x4 {
    const u32x2 lo = __builtin_bit_cast(u32x2, __builtin_amdgcn_ds_read_tr16_b64_v4i16((lds_tr_t)(wl + (unsigned)(m * 8192 + (4 * sx + (tt >> 1)) * 1024 + (tt & 1) * 8))));
    const u32x2 hi = __builtin_bit_cast(u32x2, __builtin_amdgcn_ds_read_tr16_b64_v4i16((lds_tr_t)(wl + (unsigned)(m * 8192 + (4 * sx + 2 + (tt >> 1)) * 1024 + (tt & 1) * 8))));
    return u32x4{lo.x, lo.y, hi.x, hi.y};
  };
  auto calcq = [&]() {
    u32x4 wf[8];
#pragma unroll
    for (int i = 0; i < 8; ++i) wf[i] = wfrag(0, i >> 1, i & 1);
    const u32x4 xa0 = *LDS_PTR(u32x4, ar0 + (obase - sbase)), xa1 = *LDS_PTR(u32x4, ar1 + (obase - sbase));
    const f32x4 z = {0.f, 0.f, 0.f, 0.f};
    f32x4 cq[4];
#pragma unroll
    for (int tt = 0; tt < 4; ++tt) cq[tt] = mfma32(wf[2 * tt + 1], xa1, mfma32(wf[2 * tt], xa0, z));
    q0.x = cvtpk(cq[0][0], cq[0][1]); q0.y = cvtpk(cq[0][2], cq[0][3]);
    q0.z = cvtpk(cq[1][0], cq[1][1]); q0.w = cvtpk(cq[1][2], cq[1][3]);
    q1.x = cvtpk(cq[2][0], cq[2][1]); q1.y = cvtpk(cq[2][2], cq[2][3]);
    q1.z = cvtpk(cq[3][0], cq[3][1]); q1.w = cvtpk(cq[3][2], cq[3][3]);
  };
  asm volatile("s_waitcnt vmcnt(0)" : "+v"(ca) :: "memory");
  STAMP(2);
  const unsigned kTrash = (unsigned)(size_t)(lds_void_t)(ctr + 129) >> 2;
  constexpr unsigned kInc = 0x40001u, kLim = (unsigned)kSub << 18;
  auto cidx = [&](unsigned e) -> unsigned { return (e >> 23) & 0x7fu; };
  {
    unsigned fa = c < NC ? quad_xor(ca) ^ nonce : 0u;
    while (__builtin_amdgcn_ballot_w64(fa >= 4u) != 0ull) {
      if ((unsigned)__builtin_amdgcn_s_memrealtime() - t0 > TMO) break;
      if (fa >= 4u) GLOADW(ca, cp);
      fa = c < NC ? quad_xor(ca) ^ nonce : 0u;
    }
    STAMP(10);
    const u32x4* cq = fa == 1u ? cp + regB : cp;
    GLOADC(cb, cq);
    wdma();
    auto bin4 = [&](const u32x4& ev) {
      unsigned r[4];
#pragma unroll
      for (int i = 0; i < 4; ++i) {
        r[i] = kTrash;
        if (ev[i] != dummy) r[i] = atomicAdd(&ctr[cidx(ev[i])], kInc);
      }
      const unsigned m01 = r[0] > r[1] ? r[0] : r[1], m23 = r[2] > r[3] ? r[2] : r[3];
      if (__builtin_expect((m01 > m23 ? m01 : m23) >= kLim, 0)) {
        ctr[128] = 0xff000000u;
#pragma unroll
        for (int i = 0; i < 4; ++i)
          if (r[i] >= kLim) r[i] = kTrash;
      }
#pragma unroll
      for (int i = 0; i < 4; ++i) *LDS_PTR(unsigned, (r[i] & 0xffffu) << 2) = ev[i];
    };
    if (p == 0) ca.x = dummy;
    if (c < NC && fa < 2u) bin4(ca);
    if (__builtin_expect(fa >= 2u && p == 0, 0)) L.miss[atomicAdd(&ctr[130], 1u)] = (unsigned)c;
    asm volatile("s_waitcnt vmcnt(1)" : "+v"(cb) :: "memory");
    unsigned fb = fa == 1u ? quad_xor(cb) ^ nonce : 0u;
    while (__builtin_expect(__builtin_amdgcn_ballot_w64(fb != 0u) != 0ull, 0)) {
      if ((unsigned)__builtin_amdgcn_s_memrealtime() - t0 > 2u * TMO) { ctr[128] = 0xff000000u; break; }
      if (fb != 0u) GLOADW(cb, cq);
      fb = fa == 1u ? quad_xor(cb) ^ nonce : 0u;
    }
    if (p == 3) cb.w = dummy;
    if (fa == 1u && fb == 0u) bin4(cb);
  }
  xdma();
  WAIT_VM(2);
  __builtin_amdgcn_s_setprio(P1);
  __syncthreads();
  STAMP(3);
  u32x4 c0, c1;
  unsigned fl, w[8];
  unsigned cad = (unsigned)(size_t)(lds_void_t)(L.ctr) + (unsigned)wave * 32u, fad = (unsigned)(size_t)(lds_void_t)(ctr + 128);
  asm("" : "+v"(cad), "+v"(fad));
  auto rdsub = [&]() {
    c0 = *LDS_PTR(u32x4, cad); c1 = *LDS_PTR(u32x4, cad + 16u);
    fl = *LDS_PTR(unsigned, fad);
#pragma unroll
    for (int g = 0; g < 8; ++g) w[g] = *LDS_PTR(unsigned, sl + (unsigned)g * (kSub * 4u));
  };
  {
    const unsigned nmv = *LDS_PTR(unsigned, fad + 8u);
    rdsub();
    __builtin_amdgcn_sched_barrier(0);
    const int nm = __builtin_amdgcn_readfirstlane((int)nmv);
    if (__builtin_expect(nm != 0, 0)) {
      for (int i = 0; i < nm; ++i) {
        const int e0 = (int)L.miss[i] * EC;
        int m = E - e0; m = m < EC ? m : EC;
        for (int j = t; j < m; j += 1024) {
          const unsigned d = (unsigned)dst[e0 + j];
          if (d - (unsigned)node0 < (unsigned)BN && *LDS_PTR(volatile unsigned, (kTrash << 2) - 4u) == 0u) {
            const unsigned e = mkent(tabv(d, (unsigned)b), (unsigned)src[e0 + j], L.hist[b]);
            const unsigned r = atomicAdd(&ctr[cidx(e)], kInc);
            if (r < kLim) *LDS_PTR(unsigned, (r & 0xffffu) << 2) = e;
            else ctr[128] = 0xff000000u;
          }
        }
      }
      __syncthreads();
      rdsub();
    }
  }
  bool slow = false, dense;
  int np;
  {
    unsigned a[9];
    const unsigned sll = sl + (lbase - sbase);
    a[0] = sll;
#pragma unroll
    for (int g = 0; g < 8; ++g) a[g + 1] = a[g] + ((g < 4 ? c0[g & 3] : c1[g & 3]) >> 16);
    const unsigned big = (unsigned)__builtin_amdgcn_readfirstlane((int)(c0.x | c0.y | c0.z | c0.w | c1.x | c1.y | c1.z | c1.w | fl));
    const unsigned tot4 = (unsigned)__builtin_amdgcn_readfirstlane((int)a[8]) - lbase;
    asm volatile("" :: "v"(w[0]), "v"(w[1]), "v"(w[2]), "v"(w[3]), "v"(w[4]), "v"(w[5]), "v"(w[6]), "v"(w[7]));
    np = (int)(tot4 + 124u) >> 7;
    dense = tot4 > 92u * (unsigned)(we - ws);
    if (__builtin_expect(big < 0x1000000u && tot4 <= (unsigned)kWCap * 4u, 1)) {
#pragma unroll
      for (int g = 0; g < 8; ++g) *LDS_PTR(volatile unsigned, a[g]) = w[g] & 0xc07fffffu;
      *LDS_PTR(volatile unsigned, a[8]) = kNone;
    } else {
      slow = __builtin_amdgcn_readfirstlane((int)fl) != 0 || tot4 > (unsigned)kWCap * 4u;
      if (slow) {
        np = 0;
        *LDS_PTR(volatile unsigned, sll) = kNone;
      } else {
#pragma unroll
        for (int g = 0; g < 8; ++g) {
          *LDS_PTR(volatile unsigned, a[g]) = w[g] & 0xc07fffffu;
          if (a[g + 1] - a[g] > 256u)
            *LDS_PTR(volatile unsigned, a[g] + 256u) = *LDS_PTR(volatile unsigned, sl + (unsigned)g * (kSub * 4u) + 256u) & 0xc07fffffu;
        }
        *LDS_PTR(volatile unsigned, a[8]) = kNone;
      }
    }
  }
  auto padr = [&](int j) -> unsigned { return (unsigned)j * 128u; };
  f32x4 acc[4];
#pragma unroll
  for (int i = 0; i < 4; ++i) acc[i] = f32x4{0.f, 0.f, 0.f, 0.f};

  auto dma = [&](unsigned la, unsigned e) {
#define EXP_MASK 0x00ffff80u
    const unsigned o = (e & EXP_MASK) | koff;
    asm volatile("s_mov_b32 m0, %0\n\ts_nop 0\n\tbuffer_load_dwordx4 %1, %2, 0 offen lds" :: "s"(la), "v"(o), "s"(rs) : "memory");
  };
  auto issue = [&](unsigned boff, const u32x4 e) {
    dma(sbase + boff, e.x); dma(sbase + boff + 1024, e.y); dma(sbase + boff + 2048, e.z); dma(sbase + boff + 3072, e.w);
  };
  auto fetch = [&](unsigned boff, Xd& X) {
    X.a0 = *LDS_PTR(u32x4, ar0 + boff); X.b0 = *LDS_PTR(u32x4, ar1 + boff);
    X.a1 = *LDS_PTR(u32x4, ar0 + boff + 2048); X.b1 = *LDS_PTR(u32x4, ar1 + boff + 2048);
#pragma unroll
    for (int nt = 0; nt < 4; ++nt) {
      X.t[2 * nt]     = __builtin_bit_cast(u32x2, __builtin_amdgcn_ds_read_tr16_b64_v4i16((lds_tr_t)(at[nt] + boff)));
      X.t[2 * nt + 1] = __builtin_bit_cast(u32x2, __builtin_amdgcn_ds_read_tr16_b64_v4i16((lds_tr_t)(at[nt] + boff + 2048)));
    }
  };
  auto logits = [&](const Xd& X, f32x4& l0, f32x4& l1) {
    const f32x4 z = {0.f, 0.f, 0.f, 0.f};
    l0 = mfma32(q1, X.b0, mfma32(q0, X.a0, z));
    l1 = mfma32(q1, X.b1, mfma32(q0, X.a1, z));
  };
  auto rest = [&](const Xd& X, const f32x4& l0, const f32x4& l1, unsigned e0, unsigned e1) {
    const f32x4 z = {0.f, 0.f, 0.f, 0.f};
    const float d0 = sel4(l0, e0), d1 = sel4(l1, e1);
    const unsigned pk = cvtpk(d0, d1);
    f32x2 lg = {__builtin_bit_cast(float, pk << 16), __builtin_bit_cast(float, pk & 0xffff0000u)};
    const f32x2 cz = {-0.125f * 1.4426950408889634f, -0.125f * 1.4426950408889634f};
    lg = lg * cz;
    f32x2 ex = {__builtin_amdgcn_exp2f(lg.x), __builtin_amdgcn_exp2f(lg.y)};
    const f32x2 one = {1.0f, 1.0f};
    ex = ex + one;
    const float r0 = __builtin_amdgcn_rcpf(ex.x), r1 = __builtin_amdgcn_rcpf(ex.y);
    const bool o0 = (e0 >> 24) == tsel, o1 = (e1 >> 24) == tsel;
    const unsigned long long h0 = (unsigned long long)cvtpk(o0 ? r0 : 0.f, 0.f) << (e0 & 63u);
    const unsigned long long h1 = (unsigned long long)cvtpk(o1 ? r1 : 0.f, 0.f) << (e1 & 63u);
    const f32x4 t0 = mfma16(__builtin_bit_cast(u32x2, h0), ident, z);
    const f32x4 t1 = mfma16(__builtin_bit_cast(u32x2, h1), ident, z);
    const u32x4 al = {cvtpk(t0[0], t0[1]), cvtpk(t0[2], t0[3]), cvtpk(t1[0], t1[1]), cvtpk(t1[2], t1[3])};
#pragma unroll
    for (int nt = 0; nt < 4; ++nt) {
      const u32x4 xt = {X.t[2 * nt].x, X.t[2 * nt].y, X.t[2 * nt + 1].x, X.t[2 * nt + 1].y};
      acc[nt] = mfma32(xt, al, acc[nt]);
    }
  };
  auto compute = [&](const Xd& X, unsigned e0, unsigned e1) {
    f32x4 l0, l1;
    logits(X, l0, l1);
    rest(X, l0, l1, e0, e1);
  };
  auto body = [&](unsigned bo, int j) {
    const u32x4 nx = *LDS_PTR(u32x4, padr(j + 1) + ln);
    const unsigned pa = padr(j) + lm;
    const unsigned m0 = *LDS_PTR(unsigned, pa), m1 = *LDS_PTR(unsigned, pa + 8u);
    Xd X; f32x4 l0, l1;
    __builtin_amdgcn_s_setprio(P2);
    WAIT_VM(0);
    fetch(bo, X);
    __builtin_amdgcn_sched_barrier(0);
    logits(X, l0, l1);
    WAIT_LGKM0();
    issue(bo, nx);
    __builtin_amdgcn_s_setprio(0);
    __builtin_amdgcn_sched_barrier(0);
    rest(X, l0, l1, m0, m1);
  };
  issue(0u, *LDS_PTR(u32x4, ln));
  __builtin_amdgcn_s_setprio(0);
#if !(XPOS == 0 && WPOS == 0)
  WAIT_VM(4);
  calcq();
#endif
  STAMP(6);
  int pp = 0;
  for (; pp + 1 < np; ++pp) body(0u, pp);
  u32x4 wf[8];
#pragma unroll
  for (int i = 0; i < 8; ++i) wf[i] = wfrag(1, i >> 1, i & 1);
  asm volatile("" :: "v"(wf[0]), "v"(wf[1]), "v"(wf[2]), "v"(wf[3]), "v"(wf[4]), "v"(wf[5]), "v"(wf[6]), "v"(wf[7]));
  if (pp < np) {
    const unsigned pa = padr(pp) + lm;
    const unsigned m0 = *LDS_PTR(unsigned, pa), m1 = *LDS_PTR(unsigned, pa + 8u);
    Xd X;
    WAIT_VM(0);
    fetch(0u, X);
    __builtin_amdgcn_sched_barrier(0);
    compute(X, m0, m1);
  }
  STAMP(5);
  if (__builtin_expect(slow, 0)) {
    int e0 = 0;
    while (e0 < E) {
      unsigned fill = 0u;
      for (; e0 < E && fill <= (unsigned)(kWCap - 64); e0 += 64) {
        const int e = e0 + lane;
        const unsigned dl = e < E ? (unsigned)dst[e] - (unsigned)(node0 + ws) : 0xffffffffu;
        const bool mt = dl < (unsigned)(we - ws);
        const unsigned long long mk = __builtin_amdgcn_ballot_w64(mt);
        if (mk == 0ull) continue;
        if (mt) {
          const unsigned r = fill + __builtin_amdgcn_mbcnt_hi((unsigned)(mk >> 32), __builtin_amdgcn_mbcnt_lo((unsigned)mk, 0u));
          *LDS_PTR(volatile unsigned, lbase + r * 4u) = ((dl >> 2) << 30) | ((unsigned)src[e] << 7) | ((dl & 3u) << 4);
        }
        fill += (unsigned)__builtin_popcountll(mk);
      }
      *LDS_PTR(volatile unsigned, sl + (lbase - sbase) + fill * 4u) = kNone;
      for (unsigned j = 0; j * 32u < fill; ++j) {
        const u32x4 nx = *LDS_PTR(volatile u32x4, padr((int)j) + ln);
        const unsigned m0 = *LDS_PTR(volatile unsigned, padr((int)j) + lm), m1 = *LDS_PTR(volatile unsigned, padr((int)j) + lm + 8u);
        Xd X;
        WAIT_VM(0);
        WAIT_LGKM0();
        issue(0u, nx);
        WAIT_VM(0);
        fetch(0u, X);
        compute(X, m0, m1);
      }
      WAIT_LGKM0();
    }
  }
  WAIT_VM(0);
  {
    u32x4 bh[2];
#pragma unroll
    for (int h = 0; h < 2; ++h)
#pragma unroll
      for (int k = 0; k < 4; ++k) bh[h][k] = cvtpk(acc[2 * h + (k >> 1)][2 * (k & 1)], acc[2 * h + (k >> 1)][2 * (k & 1) + 1]);
    f32x4 co[4];
#pragma unroll
    for (int tt = 0; tt < 4; ++tt) {
      const f32x4 z = {0.f, 0.f, 0.f, 0.f};
      co[tt] = mfma32(wf[2 * tt + 1], bh[1], mfma32(wf[2 * tt], bh[0], z));
    }
    if (__builtin_expect(slow || dense, 0)) {
      u32x4 bl[2];
#pragma unroll
      for (int h = 0; h < 2; ++h) {
#pragma unroll
        for (int k = 0; k < 4; ++k) {
          const float s0 = acc[2 * h + (k >> 1)][2 * (k & 1)], s1 = acc[2 * h + (k >> 1)][2 * (k & 1) + 1];
          const float f0 = __builtin_bit_cast(float, bh[h][k] << 16), f1 = __builtin_bit_cast(float, bh[h][k] & 0xffff0000u);
          bl[h][k] = cvtpk(s0 - f0, s1 - f1);
        }
      }
#pragma unroll
      for (int tt = 0; tt < 4; ++tt) co[tt] = mfma32(wf[2 * tt + 1], bl[1], mfma32(wf[2 * tt], bl[0], co[tt]));
    }
    u32x4 p0, p1;
    p0.x = cvtpk(co[0][0], co[0][1]); p0.y = cvtpk(co[0][2], co[0][3]);
    p0.z = cvtpk(co[1][0], co[1][1]); p0.w = cvtpk(co[1][2], co[1][3]);
    p1.x = cvtpk(co[2][0], co[2][1]); p1.y = cvtpk(co[2][2], co[2][3]);
    p1.z = cvtpk(co[3][0], co[3][1]); p1.w = cvtpk(co[3][2], co[3][3]);
    const unsigned ow = sbase + (unsigned)(mn * 128 + ((2 * kq) ^ (mn & 7)) * 16);
    *LDS_PTR(u32x4, ow) = p0; *LDS_PTR(u32x4, ow ^ 16u) = p1;
    const u32x4 r0 = *LDS_PTR(u32x4, sbase + (unsigned)lane * 16u), r1 = *LDS_PTR(u32x4, sbase + 1024u + (unsigned)lane * 16u);
    int lim = N - node0; lim = lim < we ? lim : we;
    unsigned char* op = reinterpret_cast<unsigned char*>(out) + (size_t)(node0 + ws + lr) * 128u + koff;
    if (ws + lr < lim) asm volatile("global_store_dwordx4 %0, %1, off sc1\n\ts_nop 2" :: "v"(op), "v"(r0) : "memory");
    if (ws + lr + 8 < lim) asm volatile("global_store_dwordx4 %0, %1, off offset:1024 sc1\n\ts_nop 2" :: "v"(op), "v"(r1) : "memory");
  }
  STAMP(7);
}

__device__ __forceinline__ float bf2f(unsigned short v) { return __builtin_bit_cast(float, (unsigned)v << 16); }

__global__ __launch_bounds__(256) void f_prep(const unsigned short* __restrict__ x, const unsigned short* __restrict__ wq,
                                              int N, unsigned short* __restrict__ Q, float* __restrict__ S) {
  const size_t i = (size_t)blockIdx.x * 256 + threadIdx.x;
  if (i >= (size_t)N * kD) return;
  const size_t n = i >> 6; const int f = (int)(i & 63);
  float a = 0.f;
  for (int k = 0; k < kD; ++k) a = fmaf(bf2f(x[n * kD + k]), bf2f(wq[k * kD + f]), a);
  Q[i] = (unsigned short)(cvtpk(a, a) & 0xffffu);
  S[i] = 0.f;
}
__global__ __launch_bounds__(256) void f_edge(const unsigned short* __restrict__ x, const unsigned short* __restrict__ Q,
                                              const int* __restrict__ src, const int* __restrict__ dst, int E,
                                              float* __restrict__ S) {
  const size_t gid = ((size_t)blockIdx.x * 256 + threadIdx.x) >> 3;
  const int p = threadIdx.x & 7;
  const bool ok = gid < (size_t)E;
  const size_t e = ok ? gid : 0;
  const int s = src[e], d = dst[e];
  const u32x4 xr = *reinterpret_cast<const u32x4*>(x + (size_t)s * kD + p * 8);
  const u32x4 qv = *reinterpret_cast<const u32x4*>(Q + (size_t)d * kD + p * 8);
  float dd = dot2(xr.x, qv.x, 0.0f);
  dd = dot2(xr.y, qv.y, dd); dd = dot2(xr.z, qv.z, dd); dd = dot2(xr.w, qv.w, dd);
  dd = group8_sum(dd);
  const unsigned al = alpha_pk(dd), ah = al << 16;
  if (ok) {
    float* o = S + (size_t)d * kD + p * 8;
    atomicAdd(o + 0, dot2(xr.x, al, 0.f)); atomicAdd(o + 1, dot2(xr.x, ah, 0.f));
    atomicAdd(o + 2, dot2(xr.y, al, 0.f)); atomicAdd(o + 3, dot2(xr.y, ah, 0.f));
    atomicAdd(o + 4, dot2(xr.z, al, 0.f)); atomicAdd(o + 5, dot2(xr.z, ah, 0.f));
    atomicAdd(o + 6, dot2(xr.w, al, 0.f)); atomicAdd(o + 7, dot2(xr.w, ah, 0.f));
  }
}
__global__ __launch_bounds__(256) void f_out(const float* __restrict__ S, const unsigned short* __restrict__ wv,
                                             int N, unsigned short* __restrict__ out) {
  const size_t i = (size_t)blockIdx.x * 256 + threadIdx.x;
  if (i >= (size_t)N * kD) return;
  const size_t n = i >> 6; const int f = (int)(i & 63);
  float a = 0.f;
  for (int k = 0; k < kD; ++k) a = fmaf(S[n * kD + k], bf2f(wv[k * kD + f]), a);
  out[i] = (unsigned short)(cvtpk(a, a) & 0xffffu);
}

extern "C" void kernel_launch(void* const* d_in, const int* in_sizes, int n_in,
                              void* d_out, int out_size, void* d_ws, size_t ws_size,
                              hipStream_t stream) {
  const unsigned short* x  = (const unsigned short*)d_in[0];
  const int* src = (const int*)d_in[1];
  const int* dst = (const int*)d_in[2];
  const unsigned short* wq = (const unsigned short*)d_in[3];
  const unsigned short* wv = (const unsigned short*)d_in[4];
  unsigned short* out = (unsigned short*)d_out;
  const int N = in_sizes[0] / kD;
  const int E = in_sizes[1];
  if (N <= 0) return;

  int BN = (N + 255) / 256; if (BN < 2) BN = 2;
  const int NB = (N + BN - 1) / BN;
  int NC = kMaxNC;
  int EC = (E + NC - 1) / NC; EC = (EC + 3) & ~3; if (EC < 4) EC = 4;
  NC = E > 0 ? (E + EC - 1) / EC : 1;
  const size_t w_cells = (size_t)((NB + 1) >> 1) * NC * 64;
  const bool fast = (BN <= kMaxBN) && (NB <= kMaxNB) && (EC <= kMaxEC) && (E >= 4) && (w_cells * 4 <= ws_size);
  if (fast) {
    const unsigned magic = (1u << 24) / (unsigned)BN + 1u;
    unsigned cmul = (1u << 26) / (unsigned)N; if (cmul > 0xffffffu) cmul = 0xffffffu;
    const unsigned rmul = (1u << 26) / (unsigned)((NB + 7) & ~7);
#define ZF 14, 14, 12, 9
    const int zf[4] = {ZF}, S4 = (BN + 3) / 4, zs = zf[0] + zf[1] + zf[2] + zf[3];
    int z[4], sum = 0;
    for (int g = 0; g < 4; ++g) { z[g] = (S4 * zf[g] + zs / 2) / zs; if (z[g] > 16) z[g] = 16; sum += z[g]; }
    for (int g = 0; sum < S4; g = (g + 1) & 3) if (z[g] < 16) { ++z[g]; ++sum; }
    for (int g = 3; sum > S4; g = (g + 3) & 3) if (z[g] > 0) { --z[g]; --sum; }
    const unsigned zp = (unsigned)z[0] | ((unsigned)z[1] << 8) | ((unsigned)z[2] << 16) | ((unsigned)z[3] << 24);
    k_main<<<NB > NC ? NB : NC, 1024, 0, stream>>>(x, src, dst, wq, wv, (u32x4*)d_ws, out, E, EC, NC, N, BN, magic, NB, cmul, rmul, zp);
  } else {
    float* S = (float*)d_ws;
    unsigned short* Q = (unsigned short*)(S + (size_t)N * kD);
    const unsigned gn = (unsigned)(((size_t)N * kD + 255) / 256);
    f_prep<<<gn, 256, 0, stream>>>(x, wq, N, Q, S);
    if (E > 0) f_edge<<<(unsigned)(((size_t)E * 8 + 255) / 256), 256, 0, stream>>>(x, Q, src, dst, E, S);
    f_out<<<gn, 256, 0, stream>>>(S, wv, N, out);
  }
}
```
